```python
import torch

def _softmax(x, axis=-1):
    return torch.softmax(x, dim=axis)
def _gelu(x):
    return torch.nn.functional.gelu(x, approximate="tanh")
def _silu(x):
    return torch.nn.functional.silu(x)
def _sigmoid(x):
    return torch.sigmoid(x)
def _layernorm(x, axis=-1, eps=1e-5):
    m = x.mean(dim=axis, keepdim=True)
    v = x.var(dim=axis, keepdim=True, unbiased=False)
    return (x - m) / torch.sqrt(v + eps)
def _scatter_add(tgt, idx, src, axis=0):
    return tgt.clone().index_add_(axis, idx, src)
def _segment_sum(src, seg, n):
    out = torch.zeros((n,) + src.shape[1:], dtype=src.dtype)
    return out.index_add_(0, seg, src)
def _pad(x, pw):
    flat = tuple(v for pair in reversed(pw) for v in pair)
    return torch.nn.functional.pad(x, flat)

kernel_name = 'concat_heads__mha_block_cached__compressed_kv_cla__decode'

def setup_inputs():

    _w = (lambda a: (lambda t: t.to(torch.bfloat16) if t.is_floating_point() else t)(torch.as_tensor(a)))
    def _A_setup():
        B, Sq, D = 2, 1, 448
        H, Sk, Dh = 16, 4096, 256
        return {
            "x": _w((lambda *s: torch.randn(*s, dtype=torch.bfloat16) * 1.6)(B, Sq, D)),
            "k_cache": _w((lambda *s: torch.randn(*s, dtype=torch.bfloat16) * 1.6)(B, H, Sk, Dh)),
            "v_cache": _w((lambda *s: torch.randn(*s, dtype=torch.bfloat16) * 0.05)(B, H, Sk, Dh)),
            "wq": _w((lambda *s: torch.randn(*s, dtype=torch.bfloat16) * 1.6)(D, H * Dh) * (D ** -0.5)),
            "wo": _w((lambda *s: torch.randn(*s, dtype=torch.bfloat16) * 0.05)(H * Dh, D) * ((H * Dh) ** -0.5)),
        }
    _A_in = _A_setup()

    def _B_setup():
        B, H, Sq, Sk, D, M = 2, 16, 1, 4096, 256, 448
        Dc = 4 * D
        return {
            "x": _w((lambda *s: torch.randn(*s, dtype=torch.bfloat16) * 3.2)(B, Sq, M)),
            "c_kv": _w((lambda *s: torch.randn(*s, dtype=torch.bfloat16) * 0.1)(B, Sk, Dc)),
            "w_q": _w((lambda *s: torch.randn(*s, dtype=torch.bfloat16) * 6.4)(M, H, D) * (M ** -0.5)),
            "w_uk": _w((lambda *s: torch.randn(*s, dtype=torch.bfloat16) * 6.4)(Dc, H, D) * (Dc ** -0.5)),
            "w_uv": _w((lambda *s: torch.randn(*s, dtype=torch.bfloat16) * 0.1)(Dc, H, D) * (Dc ** -0.5)),
            "w_o": _w((lambda *s: torch.randn(*s, dtype=torch.bfloat16) * 0.1)(H, D, M) * ((H * D) ** -0.5)),
        }
    _B_in = _B_setup()

    return {
        "A_x": _A_in["x"],
        "A_k_cache": _A_in["k_cache"],
        "A_v_cache": _A_in["v_cache"],
        "A_wq": _A_in["wq"],
        "A_wo": _A_in["wo"],
        "B_x": _B_in["x"],
        "B_c_kv": _B_in["c_kv"],
        "B_w_q": _B_in["w_q"],
        "B_w_uk": _B_in["w_uk"],
        "B_w_uv": _B_in["w_uv"],
        "B_w_o": _B_in["w_o"],
    }

def reference(A_x, A_k_cache, A_v_cache, A_wq, A_wo, B_x, B_c_kv, B_w_q, B_w_uk, B_w_uv, B_w_o):
    def _A_ref(x, k_cache, v_cache, wq, wo):
        B, Sq, D = x.shape
        H, Dh = 16, 256
        q = (lambda x, *d: x.permute(*d))((x @ wq).reshape(B, Sq, H, Dh), 0, 2, 1, 3)
        scores = torch.einsum("bhqd,bhkd->bhqk", q, k_cache) * (Dh ** -0.5)
        p = _softmax(scores, axis=-1)
        o = torch.einsum("bhqk,bhkd->bhqd", p, v_cache)
        return (lambda x, *d: x.permute(*d))(o, 0, 2, 1, 3).reshape(B, Sq, H * Dh) @ wo

    def _B_ref(x, c_kv, w_q, w_uk, w_uv, w_o):
        B, Sq, M = x.shape[0], x.shape[1], x.shape[2]
        Sk = c_kv.shape[1]
        H, D = w_q.shape[1], w_q.shape[2]
        q = torch.einsum("bqm,mhd->bhqd", x, w_q)
        k = torch.einsum("bkc,chd->bhkd", c_kv, w_uk)
        v = torch.einsum("bkc,chd->bhkd", c_kv, w_uv)
        scores = torch.einsum("bhqd,bhkd->bhqk", q, k) * (D ** -0.5)
        off = Sk - Sq
        qi = torch.arange(Sq)[:, None]
        kj = torch.arange(Sk)[None, :]
        cmask = kj <= (qi + off)
        scores = torch.where(cmask[None, None], scores, -1e9)
        p = _softmax(scores, axis=-1)
        ctx = torch.einsum("bhqk,bhkd->bhqd", p, v)
        return torch.einsum("bhqd,hdm->bqm", ctx, w_o)

    a = _A_ref(A_x, A_k_cache, A_v_cache, A_wq, A_wo)
    b = _B_ref(B_x, B_c_kv, B_w_q, B_w_uk, B_w_uv, B_w_o)
    return torch.concatenate([a, b], axis=-1)
```

```cpp
#include <hip/hip_runtime.h>
#include <hip/hip_bf16.h>
#include <stdint.h>
#define RND 0
typedef unsigned short u16;
__device__ __forceinline__ float bf2f(u16 v){ return __uint_as_float(((unsigned)v)<<16); }
__device__ __forceinline__ float rbf(float f){
  return f;
}
__device__ __forceinline__ u16 f2bf(float f){ unsigned u = __float_as_uint(f); u = (u + 0x7FFFu + ((u>>16)&1u)); return (u16)(u>>16); }

__global__ void k_q(const u16* x, const u16* w, float* q){
  int n = blockIdx.x*blockDim.x + threadIdx.x;
  float a0=0,a1=0;
  for(int m=0;m<448;m++){ float wv = bf2f(w[(size_t)m*4096+n]); a0 += bf2f(x[m])*wv; a1 += bf2f(x[448+m])*wv; }
  q[n]=rbf(a0); q[4096+n]=rbf(a1);
}
__global__ void k_qabs(const float* q, const u16* wuk, float* qabs){
  int row = blockIdx.x;
  int c = row>>4, h = row&15; int l = threadIdx.x;
  float a0=0,a1=0;
  for(int j=0;j<4;j++){ int d = l*4+j; float wv = bf2f(wuk[(size_t)row*256+d]); a0 += q[h*256+d]*wv; a1 += q[4096+h*256+d]*wv; }
  for(int o=32;o>0;o>>=1){ a0 += __shfl_down(a0,o); a1 += __shfl_down(a1,o);}
  if(l==0){ qabs[(0*16+h)*1024+c]=a0; qabs[(1*16+h)*1024+c]=a1; }
}
__global__ void k_scoresA(const float* q, const u16* K, float* s){
  int row = blockIdx.x*4 + (threadIdx.x>>6);
  int bh = row>>12; int l = threadIdx.x&63;
  float a=0;
  for(int j=0;j<4;j++){ int d=l*4+j; a += q[bh*256+d]*bf2f(K[(size_t)row*256+d]); }
  for(int o=32;o>0;o>>=1) a += __shfl_down(a,o);
  if(l==0) s[row] = rbf(rbf(a)*0.0625f);
}
__global__ void k_scoresB(const float* qabs, const u16* ckv, float* s){
  int row = blockIdx.x;
  int b = row>>12, k = row&4095; int l = threadIdx.x;
  float cv[16];
  for(int j=0;j<16;j++) cv[j] = bf2f(ckv[(size_t)row*1024 + l*16+j]);
  for(int h=0;h<16;h++){
    float a=0; for(int j=0;j<16;j++) a += qabs[(b*16+h)*1024+l*16+j]*cv[j];
    for(int o=32;o>0;o>>=1) a += __shfl_down(a,o);
    if(l==0) s[(b*16+h)*4096+k] = rbf(rbf(a)*0.0625f);
  }
}
__global__ void k_softmax(float* s){
  __shared__ float red[256];
  float* r = s + (size_t)blockIdx.x*4096; int t=threadIdx.x;
  float m=-INFINITY; for(int i=t;i<4096;i+=256) m=fmaxf(m,r[i]);
  red[t]=m; __syncthreads(); for(int o=128;o>0;o>>=1){ if(t<o) red[t]=fmaxf(red[t],red[t+o]); __syncthreads(); }
  m=red[0]; __syncthreads();
  float l=0; for(int i=t;i<4096;i+=256) l+=__expf(r[i]-m);
  red[t]=l; __syncthreads(); for(int o=128;o>0;o>>=1){ if(t<o) red[t]+=red[t+o]; __syncthreads(); }
  l=red[0];
  for(int i=t;i<4096;i+=256) r[i]=rbf(__expf(r[i]-m)/l);
}
__global__ void k_pvA(const float* p, const u16* V, float* o){
  int bh = blockIdx.x; int d = threadIdx.x; float a=0;
  for(int k=0;k<4096;k++) a += p[bh*4096+k]*bf2f(V[((size_t)bh*4096+k)*256+d]);
  o[bh*256+d]=rbf(a);
}
__global__ void k_pvB(const float* p, const u16* ckv, float* cc){
  int bh = blockIdx.x; int b = bh>>4; int c = blockIdx.y*256+threadIdx.x; float a=0;
  for(int k=0;k<4096;k++) a += p[bh*4096+k]*bf2f(ckv[((size_t)b*4096+k)*1024+c]);
  cc[bh*1024+c]=a;
}
__global__ void k_uv(const float* cc, const u16* wuv, float* ctx){
  int bh = blockIdx.x; int h = bh&15; int d = threadIdx.x; float a=0;
  for(int c=0;c<1024;c++) a += cc[bh*1024+c]*bf2f(wuv[((size_t)c*16+h)*256+d]);
  ctx[bh*256+d]=rbf(a);
}
__global__ void k_out(const float* o, const u16* wo, u16* out, int off){
  int b = blockIdx.x; int m = threadIdx.x; if(m>=448) return; float a=0;
  for(int i=0;i<4096;i++) a += o[b*4096+i]*bf2f(wo[(size_t)i*448+m]);
  out[b*896+off+m]=f2bf(a);
}
extern "C" void kernel_launch(void* const* d_in, const int* in_sizes, int n_in,
                              void* d_out, int out_size, void* d_ws, size_t ws_size,
                              hipStream_t stream) {
  const u16* Ax=(const u16*)d_in[0]; const u16* Ak=(const u16*)d_in[1]; const u16* Av=(const u16*)d_in[2];
  const u16* Awq=(const u16*)d_in[3]; const u16* Awo=(const u16*)d_in[4];
  const u16* Bx=(const u16*)d_in[5]; const u16* Bc=(const u16*)d_in[6]; const u16* Bwq=(const u16*)d_in[7];
  const u16* Bwuk=(const u16*)d_in[8]; const u16* Bwuv=(const u16*)d_in[9]; const u16* Bwo=(const u16*)d_in[10];
  float* ws=(float*)d_ws;
  float* qA=ws; float* qB=ws+8192; float* qabs=ws+16384; float* sA=qabs+32768; float* sB=sA+131072;
  float* oA=sB+131072; float* cc=oA+8192; float* ctx=cc+32768;
  u16* out=(u16*)d_out;
  k_q<<<16,256,0,stream>>>(Ax,Awq,qA);
  k_q<<<16,256,0,stream>>>(Bx,Bwq,qB);
  k_qabs<<<16384,64,0,stream>>>(qB,Bwuk,qabs);
  k_scoresA<<<32768,256,0,stream>>>(qA,Ak,sA);
  k_scoresB<<<8192,64,0,stream>>>(qabs,Bc,sB);
  k_softmax<<<64,256,0,stream>>>(sA);
  k_pvA<<<32,256,0,stream>>>(sA,Av,oA);
  k_pvB<<<dim3(32,4),256,0,stream>>>(sB,Bc,cc);
  k_uv<<<32,256,0,stream>>>(cc,Bwuv,ctx);
  k_out<<<2,448,0,stream>>>(oA,Awo,out,0);
  k_out<<<2,448,0,stream>>>(ctx,Bwo,out,448);
}
```

```cpp
#include <hip/hip_runtime.h>
#include <hip/hip_bf16.h>
#include <stdint.h>
#define RND 1
typedef unsigned short u16;
__device__ __forceinline__ float bf2f(u16 v){ return __uint_as_float(((unsigned)v)<<16); }
__device__ __forceinline__ float rbf(float f){
  unsigned u = __float_as_uint(f); u = (u + 0x7FFFu + ((u>>16)&1u)) & 0xFFFF0000u; return __uint_as_float(u);
}
__device__ __forceinline__ u16 f2bf(float f){ unsigned u = __float_as_uint(f); u = (u + 0x7FFFu + ((u>>16)&1u)); return (u16)(u>>16); }

__global__ void k_q(const u16* x, const u16* w, float* q){
  int n = blockIdx.x*blockDim.x + threadIdx.x;
  float a0=0,a1=0;
  for(int m=0;m<448;m++){ float wv = bf2f(w[(size_t)m*4096+n]); a0 += bf2f(x[m])*wv; a1 += bf2f(x[448+m])*wv; }
  q[n]=rbf(a0); q[4096+n]=rbf(a1);
}
__global__ void k_qabs(const float* q, const u16* wuk, float* qabs){
  int row = blockIdx.x;
  int c = row>>4, h = row&15; int l = threadIdx.x;
  float a0=0,a1=0;
  for(int j=0;j<4;j++){ int d = l*4+j; float wv = bf2f(wuk[(size_t)row*256+d]); a0 += q[h*256+d]*wv; a1 += q[4096+h*256+d]*wv; }
  for(int o=32;o>0;o>>=1){ a0 += __shfl_down(a0,o); a1 += __shfl_down(a1,o);}
  if(l==0){ qabs[(0*16+h)*1024+c]=a0; qabs[(1*16+h)*1024+c]=a1; }
}
__global__ void k_scoresA(const float* q, const u16* K, float* s){
  int row = blockIdx.x*4 + (threadIdx.x>>6);
  int bh = row>>12; int l = threadIdx.x&63;
  float a=0;
  for(int j=0;j<4;j++){ int d=l*4+j; a += q[bh*256+d]*bf2f(K[(size_t)row*256+d]); }
  for(int o=32;o>0;o>>=1) a += __shfl_down(a,o);
  if(l==0) s[row] = rbf(rbf(a)*0.0625f);
}
__global__ void k_scoresB(const float* qabs, const u16* ckv, float* s){
  int row = blockIdx.x;
  int b = row>>12, k = row&4095; int l = threadIdx.x;
  float cv[16];
  for(int j=0;j<16;j++) cv[j] = bf2f(ckv[(size_t)row*1024 + l*16+j]);
  for(int h=0;h<16;h++){
    float a=0; for(int j=0;j<16;j++) a += qabs[(b*16+h)*1024+l*16+j]*cv[j];
    for(int o=32;o>0;o>>=1) a += __shfl_down(a,o);
    if(l==0) s[(b*16+h)*4096+k] = rbf(rbf(a)*0.0625f);
  }
}
__global__ void k_softmax(float* s){
  __shared__ float red[256];
  float* r = s + (size_t)blockIdx.x*4096; int t=threadIdx.x;
  float m=-INFINITY; for(int i=t;i<4096;i+=256) m=fmaxf(m,r[i]);
  red[t]=m; __syncthreads(); for(int o=128;o>0;o>>=1){ if(t<o) red[t]=fmaxf(red[t],red[t+o]); __syncthreads(); }
  m=red[0]; __syncthreads();
  float l=0; for(int i=t;i<4096;i+=256) l+=__expf(r[i]-m);
  red[t]=l; __syncthreads(); for(int o=128;o>0;o>>=1){ if(t<o) red[t]+=red[t+o]; __syncthreads(); }
  l=red[0];
  for(int i=t;i<4096;i+=256) r[i]=rbf(__expf(r[i]-m)/l);
}
__global__ void k_pvA(const float* p, const u16* V, float* o){
  int bh = blockIdx.x; int d = threadIdx.x; float a=0;
  for(int k=0;k<4096;k++) a += p[bh*4096+k]*bf2f(V[((size_t)bh*4096+k)*256+d]);
  o[bh*256+d]=rbf(a);
}
__global__ void k_pvB(const float* p, const u16* ckv, float* cc){
  int bh = blockIdx.x; int b = bh>>4; int c = blockIdx.y*256+threadIdx.x; float a=0;
  for(int k=0;k<4096;k++) a += p[bh*4096+k]*bf2f(ckv[((size_t)b*4096+k)*1024+c]);
  cc[bh*1024+c]=a;
}
__global__ void k_uv(const float* cc, const u16* wuv, float* ctx){
  int bh = blockIdx.x; int h = bh&15; int d = threadIdx.x; float a=0;
  for(int c=0;c<1024;c++) a += cc[bh*1024+c]*bf2f(wuv[((size_t)c*16+h)*256+d]);
  ctx[bh*256+d]=rbf(a);
}
__global__ void k_out(const float* o, const u16* wo, u16* out, int off){
  int b = blockIdx.x; int m = threadIdx.x; if(m>=448) return; float a=0;
  for(int i=0;i<4096;i++) a += o[b*4096+i]*bf2f(wo[(size_t)i*448+m]);
  out[b*896+off+m]=f2bf(a);
}

#define KVNT 1
typedef unsigned v4u __attribute__((ext_vector_type(4)));
typedef float f32x4 __attribute__((ext_vector_type(4)));
using bf16x2 = __attribute__((ext_vector_type(2))) __bf16;
__device__ __forceinline__ float dot2(unsigned k, unsigned q, float acc) {
  return __builtin_amdgcn_fdot2_f32_bf16(__builtin_bit_cast(bf16x2, k), __builtin_bit_cast(bf16x2, q), acc, false);
}
template <int CTRL> __device__ __forceinline__ float dpp(float x) {
  return __builtin_bit_cast(float, __builtin_amdgcn_mov_dpp(__builtin_bit_cast(int, x), CTRL, 0xf, 0xf, true));
}
constexpr int XOR1 = 0xB1, XOR2 = 0x4E, XOR7 = 0x141, XOR8 = 0x128, ROR4 = 0x124;
__device__ __forceinline__ v4u ldg(const u16* p){ return KVNT ? __builtin_nontemporal_load((const v4u*)p) : *(const v4u*)p; }
__global__ void __launch_bounds__(512,2) k_attnA(const float* q, const u16* K, const u16* V, float* part){
  __shared__ __attribute__((aligned(16))) float pl[8][64];
  __shared__ __attribute__((aligned(16))) float red[8][260];
  const int bh = blockIdx.x>>3, sp = blockIdx.x&7;
  const int lane = threadIdx.x&63, wave = __builtin_amdgcn_readfirstlane(threadIdx.x>>6);
  const int half = lane>>5, l32 = lane&31;
  const size_t row0 = (size_t)bh*4096 + sp*512 + wave*64;
  const u16* kp = K + (row0+half)*256 + l32*8;
  const u16* vp = V + (row0+half)*256 + l32*8;
  v4u kb[32];
  #pragma unroll
  for(int i=0;i<32;i++) kb[i] = ldg(kp + i*512);
  unsigned qp[4];
  #pragma unroll
  for(int j=0;j<4;j++){ float a = q[bh*256+l32*8+2*j], b = q[bh*256+l32*8+2*j+1]; qp[j] = (__float_as_uint(a)>>16) | (__float_as_uint(b)&0xffff0000u); }
  v4u vb[16];
  #pragma unroll
  for(int i=0;i<16;i++) vb[i] = ldg(vp + i*512);
  float x[32];
  #pragma unroll
  for(int i=0;i<32;i++){ float d=0; for(int j=0;j<4;j++) d = dot2(kb[i][j], qp[j], d); x[i]=d; }
  v4u vc[16];
  #pragma unroll
  for(int i=0;i<16;i++) vc[i] = ldg(vp + (16+i)*512);
  #pragma unroll
  for(int i=0;i<16;i++){ auto s = __builtin_amdgcn_permlane16_swap(__float_as_uint(x[i]), __float_as_uint(x[i+16]), false, false);
    x[i] = __uint_as_float(s[0]) + __uint_as_float(s[1]); }
  const int c = lane&15;
#define TR_STEP(HALF, CTRL, BIT) _Pragma("unroll") for (int h = 0; h < (HALF); ++h) { \
      const float lo_ = x[h], hi_ = x[h + (HALF)]; const float t1_ = lo_ + dpp<CTRL>(lo_), t2_ = hi_ + dpp<CTRL>(hi_); \
      x[h] = (c & (BIT)) ? t2_ : t1_; }
  TR_STEP(8, XOR8, 8) TR_STEP(4, XOR7, 4) TR_STEP(2, XOR2, 2) TR_STEP(1, XOR1, 1)
  float s = rbf(rbf(x[0])*0.0625f);
  float m = s;
  for(int o=1;o<64;o<<=1) m = fmaxf(m, __shfl_xor(m,o));
  float p = __expf(s-m);
  float l = p;
  for(int o=1;o<64;o<<=1) l += __shfl_xor(l,o);
  pl[wave][half*32+l32] = p;
  __builtin_amdgcn_s_waitcnt(0xC07F);
  float acc[8] = {0,0,0,0,0,0,0,0};
  #pragma unroll
  for(int i4=0;i4<8;i4++){
    f32x4 pp = *(const f32x4*)&pl[wave][half*32+i4*4];
    #pragma unroll
    for(int u=0;u<4;u++){ int i=i4*4+u; v4u v = i<16 ? vb[i&15] : vc[i&15];
      #pragma unroll
      for(int j=0;j<4;j++){ acc[2*j] = fmaf(pp[u], __uint_as_float(v[j]<<16), acc[2*j]); acc[2*j+1] = fmaf(pp[u], __uint_as_float(v[j]&0xffff0000u), acc[2*j+1]); } }
  }
  #pragma unroll
  for(int j=0;j<8;j++) acc[j] += __shfl_xor(acc[j],32);
  if(half==0){ for(int j=0;j<8;j++) red[wave][4+l32*8+j]=acc[j]; if(lane==0){ red[wave][0]=m; red[wave][1]=l; } }
  __syncthreads();
  if(threadIdx.x<256){
    float M=-INFINITY; for(int w=0;w<8;w++) M=fmaxf(M,red[w][0]);
    float L=0,o=0; for(int w=0;w<8;w++){ float e=__expf(red[w][0]-M); L+=e*red[w][1]; o+=e*red[w][4+threadIdx.x]; }
    float* r = part + (size_t)blockIdx.x*260;
    r[4+threadIdx.x]=o; if(threadIdx.x==0){ r[0]=M; r[1]=L; }
  }
}
__global__ void k_combA(const float* part, float* o){
  int bh=blockIdx.x, d=threadIdx.x; const float* r = part + (size_t)bh*8*260;
  float M=-INFINITY; for(int s=0;s<8;s++) M=fmaxf(M,r[s*260]);
  float L=0,a=0; for(int s=0;s<8;s++){ float e=__expf(r[s*260]-M); L+=e*r[s*260+1]; a+=e*r[s*260+4+d]; }
  o[bh*256+d]=rbf(a/L);
}
extern "C" void kernel_launch(void* const* d_in, const int* in_sizes, int n_in,
                              void* d_out, int out_size, void* d_ws, size_t ws_size,
                              hipStream_t stream) {
  const u16* Ax=(const u16*)d_in[0]; const u16* Ak=(const u16*)d_in[1]; const u16* Av=(const u16*)d_in[2];
  const u16* Awq=(const u16*)d_in[3]; const u16* Awo=(const u16*)d_in[4];
  const u16* Bx=(const u16*)d_in[5]; const u16* Bc=(const u16*)d_in[6]; const u16* Bwq=(const u16*)d_in[7];
  const u16* Bwuk=(const u16*)d_in[8]; const u16* Bwuv=(const u16*)d_in[9]; const u16* Bwo=(const u16*)d_in[10];
  float* ws=(float*)d_ws;
  float* qA=ws; float* qB=ws+8192; float* qabs=ws+16384; float* sA=qabs+32768; float* sB=sA+131072;
  float* oA=sB+131072; float* cc=oA+8192; float* ctx=cc+32768;
  u16* out=(u16*)d_out;
  k_q<<<16,256,0,stream>>>(Ax,Awq,qA);
  float* part = ctx + 8192;
  k_attnA<<<256,512,0,stream>>>(qA,Ak,Av,part);
  k_combA<<<32,256,0,stream>>>(part,oA);
  k_q<<<16,256,0,stream>>>(Bx,Bwq,qB);
  k_qabs<<<16384,64,0,stream>>>(qB,Bwuk,qabs);
  k_scoresB<<<8192,64,0,stream>>>(qabs,Bc,sB);
  k_softmax<<<32,256,0,stream>>>(sB);
  k_pvB<<<dim3(32,4),256,0,stream>>>(sB,Bc,cc);
  k_uv<<<32,256,0,stream>>>(cc,Bwuv,ctx);
  k_out<<<2,448,0,stream>>>(oA,Awo,out,0);
  k_out<<<2,448,0,stream>>>(ctx,Bwo,out,448);
}
```

```cpp
#include <hip/hip_runtime.h>
#include <stdint.h>

typedef unsigned short u16;
typedef unsigned int u32;
typedef unsigned long long u64;
typedef u32 v4u __attribute__((ext_vector_type(4)));
typedef u32 v2u __attribute__((ext_vector_type(2)));
typedef float f32x4 __attribute__((ext_vector_type(4)));
typedef float f32x2 __attribute__((ext_vector_type(2)));
typedef __bf16 bf16x2 __attribute__((ext_vector_type(2)));
typedef __bf16 bf16x8 __attribute__((ext_vector_type(8)));
typedef short s16x4 __attribute__((ext_vector_type(4)));
typedef __amdgpu_buffer_rsrc_t rsrc_t;
#define LAS __attribute__((address_space(3)))

#define NSLOT_A 3
#define NSLOT_C 3
#define PACE_A 4
#define PACE_C 6
#define PACE_W 4

constexpr u32 WS_E = 0, WS_F2 = 4096, WS_F3 = 8192, WS_ST3 = 12288, WS_SA = 16384, WS_QG = 32768, WS_QABS = 163840,
              WS_OA = 262144, WS_OUTP = 786432, WS_P3 = 2097152, WS_BYTES = 4194304;
constexpr int L_T = 0, L_PUB = 12, L_QAF = 16, L_CNT1 = 32;
constexpr int L_S1P = 256;
constexpr int L_QA = 2304;
constexpr int L_QB = 2816;
constexpr int L_BIG = 8192;
constexpr int L_XQ = L_BIG + 65536;
constexpr int LDS_BYTES = L_XQ + 65536;
#define SPIN_CAP_V 40000
constexpr u32 SPIN_CAP = SPIN_CAP_V;
constexpr float CEXP = 0.0625f * 1.4426950408889634f;

constexpr int AUX_NT = 2, AUX_SC1 = 16, AUX_POLL = 16;
#define CFENCE() asm volatile("" ::: "memory")

__device__ __forceinline__ rsrc_t mkrsrc(const void* p, u32 bytes) {
  return __builtin_amdgcn_make_buffer_rsrc(const_cast<void*>(p), 0, bytes, 0x00020000);
}
template <int AUX> __device__ __forceinline__ v4u ld16(rsrc_t r, u32 voff, u32 soff) {
  return __builtin_amdgcn_raw_buffer_load_b128(r, voff, soff, AUX);
}
template <int AUX> __device__ __forceinline__ v2u ld8(rsrc_t r, u32 voff, u32 soff) {
  return __builtin_amdgcn_raw_buffer_load_b64(r, voff, soff, AUX);
}
__device__ __forceinline__ void st16(v4u v, rsrc_t r, u32 voff) { __builtin_amdgcn_raw_buffer_store_b128(v, r, voff, 0, AUX_SC1); }
__device__ __forceinline__ void st8(v2u v, rsrc_t r, u32 voff) { __builtin_amdgcn_raw_buffer_store_b64(v, r, voff, 0, AUX_SC1); }
__device__ __forceinline__ void st4(u32 v, rsrc_t r, u32 voff) { __builtin_amdgcn_raw_buffer_store_b32(v, r, voff, 0, AUX_SC1); }
#define WAIT_VM(n) asm volatile("s_waitcnt vmcnt(" #n ")" ::: "memory")
#define WAIT_VM_N(n) asm volatile("s_waitcnt vmcnt(%0)" :: "i"(n) : "memory")
#define WAIT_LGKM() asm volatile("s_waitcnt lgkmcnt(0)" ::: "memory")
#define BARRIER() do { WAIT_LGKM(); __builtin_amdgcn_s_barrier(); asm volatile("" ::: "memory"); } while (0)

__device__ __forceinline__ float dot2(u32 k, u32 q, float acc) {
  return __builtin_amdgcn_fdot2_f32_bf16(__builtin_bit_cast(bf16x2, k), __builtin_bit_cast(bf16x2, q), acc, false);
}
__device__ __forceinline__ float dot8(v4u k, v4u q) {
  float d = dot2(k[0], q[0], 0.f); d = dot2(k[1], q[1], d); d = dot2(k[2], q[2], d); return dot2(k[3], q[3], d);
}
__device__ __forceinline__ u32 pkbf(float lo, float hi) {
  f32x2 x = {lo, hi};
  return __builtin_bit_cast(u32, __builtin_convertvector(x, bf16x2));
}
__device__ __forceinline__ float rbf(float f) { return __uint_as_float(pkbf(f, f) & 0xffff0000u); }
__device__ __forceinline__ float blo(u32 v) { return __uint_as_float(v << 16); }
__device__ __forceinline__ float bhi(u32 v) { return __uint_as_float(v & 0xffff0000u); }
template <int CTRL> __device__ __forceinline__ float dpp(float x) {
  return __builtin_bit_cast(float, __builtin_amdgcn_mov_dpp(__builtin_bit_cast(int, x), CTRL, 0xf, 0xf, true));
}
constexpr int XOR1 = 0xB1, XOR2 = 0x4E, XOR7 = 0x141, XOR8 = 0x128;
__device__ __forceinline__ float xor4(float x) {
  int v = __builtin_bit_cast(int, x);
  int t = __builtin_amdgcn_update_dpp(0, v, 0x114, 0xf, 0xa, false);
  t = __builtin_amdgcn_update_dpp(t, v, 0x104, 0xf, 0x5, false);
  return __builtin_bit_cast(float, t);
}
__device__ __forceinline__ float tr16(float a, float b) {
  auto s = __builtin_amdgcn_permlane16_swap(__float_as_uint(a), __float_as_uint(b), false, false);
  return __uint_as_float(s[0]) + __uint_as_float(s[1]);
}
__device__ __forceinline__ float tr32(float a, float b) {
  auto s = __builtin_amdgcn_permlane32_swap(__float_as_uint(a), __float_as_uint(b), false, false);
  return __uint_as_float(s[0]) + __uint_as_float(s[1]);
}
__device__ __forceinline__ float max16(float a) {
  auto s = __builtin_amdgcn_permlane16_swap(__float_as_uint(a), __float_as_uint(a), false, false);
  return fmaxf(__uint_as_float(s[0]), __uint_as_float(s[1]));
}
__device__ __forceinline__ float max32(float a) {
  auto s = __builtin_amdgcn_permlane32_swap(__float_as_uint(a), __float_as_uint(a), false, false);
  return fmaxf(__uint_as_float(s[0]), __uint_as_float(s[1]));
}
__device__ __forceinline__ float ex2(float x) { return __builtin_amdgcn_exp2f(x); }

struct Tag { u32 lo, hi; };
__device__ __forceinline__ u32 genc(float f, Tag t) { return ((__float_as_uint(f) + 0x80u) >> 8) | t.lo; }
__device__ __forceinline__ float gdec(u32 lo) { return __uint_as_float(lo << 8); }
__device__ __forceinline__ u32 gbad(v4u g, Tag t) { return (g[1] ^ t.hi) | (g[3] ^ t.hi) | ((g[0] ^ t.lo) >> 24) | ((g[2] ^ t.lo) >> 24); }
__device__ __forceinline__ bool all0(u32 x) { return __builtin_amdgcn_ballot_w64(x != 0u) == 0ull; }

__device__ __forceinline__ int get_lane() {
  int l; asm volatile("v_mbcnt_lo_u32_b32 %0, -1, 0\n\tv_mbcnt_hi_u32_b32 %0, -1, %0" : "=v"(l)); return l;
}
constexpr u32 OOB = 0x40000000u;
__device__ __forceinline__ void lds_wait(volatile LAS u32* p, u32 target) {
#pragma unroll 1
  for (u32 spin = 0; spin < SPIN_CAP * 8u; ++spin) {
    if ((u32)__builtin_amdgcn_readfirstlane(*p) >= target) break;
    __builtin_amdgcn_s_sleep(1);
  }
  asm volatile("" ::: "memory");
}
#define STAMP(i) do {} while (0)

__device__ __forceinline__ void pair_rows(v4u (&w)[16]) {
#pragma unroll
  for (int i = 0; i < 16; i += 2)
#pragma unroll
    for (int k = 0; k < 4; ++k) {
      const u32 a = w[i][k], b = w[i + 1][k];
      w[i][k] = __builtin_amdgcn_perm(b, a, 0x05040100u);
      w[i + 1][k] = __builtin_amdgcn_perm(b, a, 0x07060302u);
    }
#pragma unroll
  for (int i = 0; i < 16; ++i) asm volatile("" : "+v"(w[i]));
}
__device__ __forceinline__ void project_publish(const v4u (&w)[16], const u32 (&op)[16], char* lds, rsrc_t rws, u32 dst, Tag tg,
                                                int wave, int lane) {
  float acc[2][8];
#pragma unroll
  for (int b = 0; b < 2; ++b)
#pragma unroll
    for (int k = 0; k < 8; ++k) acc[b][k] = 0.f;
#pragma unroll
  for (int rp = 0; rp < 8; ++rp)
#pragma unroll
    for (int b = 0; b < 2; ++b)
#pragma unroll
      for (int k = 0; k < 4; ++k) {
        acc[b][2 * k] = dot2(w[2 * rp][k], op[b * 8 + rp], acc[b][2 * k]);
        acc[b][2 * k + 1] = dot2(w[2 * rp + 1][k], op[b * 8 + rp], acc[b][2 * k + 1]);
      }
  float* red = (float*)(lds + L_BIG + 32768);
  if (lane < 56) {
#pragma unroll
    for (int k = 0; k < 4; ++k)
      *(f32x4*)(red + wave * 896 + lane * 16 + k * 4) = f32x4{acc[0][2 * k], acc[1][2 * k], acc[0][2 * k + 1], acc[1][2 * k + 1]};
  }
  BARRIER();
  const int t = wave * 64 + lane;
  if (t < 448) {
    float s0 = 0.f, s1 = 0.f;
#pragma unroll
    for (int w8 = 0; w8 < 8; ++w8) { const f32x2 v = *(const f32x2*)(red + w8 * 896 + t * 2); s0 += v[0]; s1 += v[1]; }
    st16(v4u{genc(s0, tg), tg.hi, genc(s1, tg), tg.hi}, rws, dst + t * 16);
  }
}
__device__ __forceinline__ void final_reduce(rsrc_t rws, u32 src, Tag tg, u16* out, int f, int lane) {
  const u32 off = src + (lane >> 1) * 7168 + (2 * f + (lane & 1)) * 16;
  v4u g;
#pragma unroll 1
  for (u32 spin = 0; spin < SPIN_CAP; ++spin) {
    CFENCE();
    g = ld16<AUX_POLL>(rws, off, 0);
    if (all0(gbad(g, tg))) break;
    __builtin_amdgcn_s_sleep(1);
  }
  float a = gdec(g[0]), b = gdec(g[2]);
  a = tr32(a, b);
  a = tr16(a, a);
  a += dpp<XOR8>(a);
  a += xor4(a);
  a += dpp<XOR2>(a);
  const float o = dpp<XOR1>(a);
  if ((lane & 31) == 0) *(u32*)(out + (lane >> 5) * 896 + 2 * f) = pkbf(a, o);
}

__global__ void __launch_bounds__(512, 2)
fused_decode(const u16* __restrict__ Ax, const u16* __restrict__ Ak, const u16* __restrict__ Av, const u16* __restrict__ Awq,
             const u16* __restrict__ Awo, const u16* __restrict__ Bx, const u16* __restrict__ Bc, const u16* __restrict__ Bwq,
             const u16* __restrict__ Bwuk, const u16* __restrict__ Bwuv, const u16* __restrict__ Bwo, u16* out,
             char* wsp) {
  __shared__ __attribute__((aligned(16))) char lds[LDS_BYTES];
  const int lane = threadIdx.x & 63, wave = __builtin_amdgcn_readfirstlane(threadIdx.x >> 6);
  const int g = blockIdx.x, xc = g & 7, r = g >> 3;
  const rsrc_t rws = mkrsrc(wsp, WS_BYTES);
  volatile LAS u32* ctl = (volatile LAS u32*)lds;
  STAMP(0);

  const int cb = xc * 32 + r, mat = cb >> 7, cbm = cb & 127;
  const bool roleC = r < 4, roleW = (r >= 4) && (r < 8);
  const bool s2wave = roleC ? false : (roleW ? wave < 2 : wave == 0);
  const int t2 = roleW ? 24 + 2 * (r - 4) + wave : r - 8;
  const int h2 = 2 * xc + (t2 >> 4), cs2 = t2 & 15;
  Tag tg; u64 T = 0;
  v4u wk[32];
  const rsrc_t ruk = mkrsrc(Bwuk, 1024u * 8192u);
  const u32 vuk = ((u32)(cs2 * 64 + 32 * (lane >> 5)) * 16 + h2) * 512u + (lane & 31) * 16;
  if (wave > 0) {
    v4u w1[4]; v2u x0, x1;
    const rsrc_t rw = mkrsrc(mat ? Bwq : Awq, 448u * 8192u);
    const rsrc_t rx = mkrsrc(mat ? Bx : Ax, 2u * 896u);
    const u32 row0 = 64 * (wave - 1) + 4 * (lane >> 2);
    const u32 vo = row0 * 8192u + cbm * 64 + (lane & 3) * 16;
#pragma unroll
    for (int i = 0; i < 4; ++i) w1[i] = ld16<0>(rw, vo, i * 8192);
    x0 = ld8<0>(rx, row0 * 2, 0); x1 = ld8<0>(rx, row0 * 2, 896);
    BARRIER();
    float v[16];
#pragma unroll
    for (int k = 0; k < 16; ++k) v[k] = 0.f;
#pragma unroll
    for (int i = 0; i < 4; ++i) {
      const float xa = (i & 1) ? bhi(x0[i >> 1]) : blo(x0[i >> 1]);
      const float xb = (i & 1) ? bhi(x1[i >> 1]) : blo(x1[i >> 1]);
#pragma unroll
      for (int k = 0; k < 4; ++k) {
        const float lo = blo(w1[i][k]), hi = bhi(w1[i][k]);
        v[2 * k] = fmaf(xa, lo, v[2 * k]); v[2 * k + 1] = fmaf(xa, hi, v[2 * k + 1]);
        v[8 + 2 * k] = fmaf(xb, lo, v[8 + 2 * k]); v[8 + 2 * k + 1] = fmaf(xb, hi, v[8 + 2 * k + 1]);
      }
    }
#pragma unroll
    for (int k = 0; k < 8; ++k) v[k] = tr32(v[k], v[k + 8]);
#pragma unroll
    for (int k = 0; k < 4; ++k) v[k] = tr16(v[k], v[k + 4]);
#pragma unroll
    for (int k = 0; k < 2; ++k) { const float a = v[k] + dpp<XOR8>(v[k]), b = v[k + 2] + dpp<XOR8>(v[k + 2]); v[k] = (lane & 8) ? b : a; }
    { const float a = v[0] + xor4(v[0]), b = v[1] + xor4(v[1]); v[0] = (lane & 4) ? b : a; }
    const int col = (lane & 3) * 8 + ((lane >> 4) & 1) * 4 + ((lane >> 3) & 1) * 2 + ((lane >> 2) & 1);
    ((float*)(lds + L_S1P))[(wave - 1) * 64 + (lane >> 5) * 32 + col] = v[0];
    WAIT_LGKM();
    ctl[L_CNT1 / 4 + wave] = 1;
    lds_wait(ctl + L_PUB / 4, 1u);
    T = ((u64)ctl[L_T / 4 + 1] << 32) | ctl[L_T / 4];
    if (s2wave) {
#pragma unroll
      for (int i = 0; i < 32; ++i) wk[i] = ld16<AUX_NT>(ruk, vuk, i * 8192);
    }
  } else {
    const v2u ev = ld8<AUX_POLL>(rws, WS_E, 0);
    ctl[lane & 31] = (lane & 31) == L_CNT1 / 4 ? 1u : 0u;
    BARRIER();
    const rsrc_t ruk0 = mkrsrc(Bwuk, roleC ? 0u : 1024u * 8192u);
#pragma unroll
    for (int i = 0; i < 32; ++i) wk[i] = ld16<AUX_NT>(ruk0, vuk, i * 8192);
    __builtin_amdgcn_sched_barrier(0);
    const u64 E = ((u64)__builtin_amdgcn_readfirstlane(ev[1]) << 32) | (u32)__builtin_amdgcn_readfirstlane(ev[0]);
    T = (E + 1ull) * 0x9E3779B97F4A7C15ull;
#pragma unroll 1
    for (u32 spin = 0; spin < SPIN_CAP * 8u; ++spin) {
      if (all0(ctl[L_CNT1 / 4 + (lane & 7)] ^ 1u)) break;
      __builtin_amdgcn_s_sleep(1);
    }
    asm volatile("" ::: "memory");
    float q = 0.f;
#pragma unroll
    for (int w = 0; w < 7; ++w) q += ((const float*)(lds + L_S1P))[w * 64 + lane];
    const u32 lo = (__float_as_uint(rbf(q)) >> 8) | ((u32)(T >> 24) << 24);
    st8(v2u{lo, (u32)(T >> 32)}, rws, WS_QG + ((mat * 2 + (lane >> 5)) * 4096 + cbm * 32 + (lane & 31)) * 8);
    ctl[L_T / 4] = (u32)T; ctl[L_T / 4 + 1] = (u32)(T >> 32); ctl[6] = (u32)E; ctl[7] = (u32)(E >> 32);
    WAIT_LGKM();
    ctl[L_PUB / 4] = 1;
  }
  T = ((u64)__builtin_amdgcn_readfirstlane((u32)(T >> 32)) << 32) | (u32)__builtin_amdgcn_readfirstlane((u32)T);
  tg.lo = (u32)(T >> 24) << 24; tg.hi = (u32)(T >> 32);
  STAMP(1);

  const int ai = r - 8, gi = ai / 6, ja = ai - gi * 6;
  const int hA = 2 * xc + (gi >> 1), bA = gi & 1;

  if (s2wave) {
    bool doneA = roleW, doneB = false;
    const u32 offA = WS_QG + (bA * 4096 + hA * 256) * 8 + lane * 16;
    const u32 offB = WS_QG + (2 * 4096 + h2 * 256) * 8 + lane * 16;
    u32* qb = (u32*)(lds + L_QB + wave * 1024);
#pragma unroll 1
    for (u32 spin = 0; spin < SPIN_CAP && !(doneA && doneB); ++spin) {
      v4u a0, a1, b0, b1, b2, b3;
      CFENCE();
      if (!doneA) { a0 = ld16<AUX_POLL>(rws, offA, 0); a1 = ld16<AUX_POLL>(rws, offA, 1024); }
      if (!doneB) { b0 = ld16<AUX_POLL>(rws, offB, 0); b1 = ld16<AUX_POLL>(rws, offB, 1024);
                    b2 = ld16<AUX_POLL>(rws, offB, 32768); b3 = ld16<AUX_POLL>(rws, offB, 32768 + 1024); }
      if (!doneA && all0(gbad(a0, tg) | gbad(a1, tg))) {
        u32* qa = (u32*)(lds + L_QA);
        qa[lane] = ((a0[0] >> 8) & 0xffffu) | ((a0[2] << 8) & 0xffff0000u);
        qa[64 + lane] = ((a1[0] >> 8) & 0xffffu) | ((a1[2] << 8) & 0xffff0000u);
        WAIT_LGKM();
        ctl[L_QAF / 4] = 1;
        doneA = true;
      }
      if (!doneB && all0(gbad(b0, tg) | gbad(b1, tg) | gbad(b2, tg) | gbad(b3, tg))) {
        qb[lane] = ((b0[0] >> 8) & 0xffffu) | ((b0[2] << 8) & 0xffff0000u);
        qb[64 + lane] = ((b1[0] >> 8) & 0xffffu) | ((b1[2] << 8) & 0xffff0000u);
        qb[128 + lane] = ((b2[0] >> 8) & 0xffffu) | ((b2[2] << 8) & 0xffff0000u);
        qb[192 + lane] = ((b3[0] >> 8) & 0xffffu) | ((b3[2] << 8) & 0xffff0000u);
        doneB = true;
      }
    }
    STAMP(2);
    WAIT_LGKM();
    const v4u q0 = *(const v4u*)(qb + (lane & 31) * 4), q1 = *(const v4u*)(qb + 128 + (lane & 31) * 4);
    float v[32];
#pragma unroll
    for (int i = 0; i < 32; ++i) v[i] = tr16(dot8(wk[i], q0), dot8(wk[i], q1));
#define TR_STEP(HALF, CTRL, BIT) _Pragma("unroll") for (int k = 0; k < (HALF); ++k) { \
      const float a_ = v[k] + dpp<CTRL>(v[k]), b_ = v[k + (HALF)] + dpp<CTRL>(v[k + (HALF)]); v[k] = (lane & (BIT)) ? b_ : a_; }
    TR_STEP(16, XOR8, 8) TR_STEP(8, XOR7, 4) TR_STEP(4, XOR2, 2) TR_STEP(2, XOR1, 1)
#undef TR_STEP
    st4(pkbf(v[0], v[1]), rws, WS_QABS + ((((lane >> 4) & 1) * 16 + h2) * 1024 + cs2 * 64 + 32 * (lane >> 5) + 2 * (lane & 15)) * 2);
    WAIT_VM(0);
    st8(v2u{(u32)T, (u32)(T >> 32)}, rws, lane == 0 ? WS_F2 + (h2 * 16 + cs2) * 8 : OOB);
    ctl[L_QAF / 4] = 1;
    STAMP(3);
  }

  if (r >= 8) {
    const int lane = get_lane();
    const int u0 = ja * 85 + (ja < 2 ? ja : 2), nu = 85 + (ja < 2 ? 1 : 0);
    const size_t base = ((size_t)(bA * 16 + hA) * 4096 + (size_t)u0 * 8) * 256;
    const rsrc_t rk = mkrsrc(Ak + base, nu * 4096), rv = mkrsrc(Av + base, nu * 4096);
    const int first = wave ? wave - 1 : 77, stride = wave ? 7 : 1, cnt = wave ? 11 : nu - 77;
    const u32 vo = lane * 16;
    v4u kb[NSLOT_A][4], vb[NSLOT_A][4];
#pragma unroll
    for (int s = 0; s < NSLOT_A; ++s) {
      const u32 so = (first + stride * s) * 4096;
#pragma unroll
      for (int i = 0; i < 8; ++i) {
        if ((i & 1) == 0) WAIT_VM_N(PACE_A - 2);
        if (i < 4) kb[s][i] = ld16<AUX_NT>(rk, vo + i * 1024, so);
        else vb[s][i - 4] = ld16<AUX_NT>(rv, vo + (i - 4) * 1024, so);
      }
    }
    STAMP(4);
    lds_wait(ctl + L_QAF / 4, 1u);
    STAMP(5);
    const v4u q = *(const v4u*)(lds + L_QA + (lane & 31) * 16);
    float m = -INFINITY, l = 0.f, acc[8];
#pragma unroll
    for (int k = 0; k < 8; ++k) acc[k] = 0.f;
    for (int it = 0; it < cnt; it += NSLOT_A) {
#pragma unroll
      for (int s = 0; s < NSLOT_A; ++s) {
        const int k = it + s;
        const bool more = k + NSLOT_A < cnt;
        const u32 so = more ? (u32)(first + stride * (k + NSLOT_A)) * 4096u : OOB;
        float p0 = 0.f, p1 = 0.f, p2 = 0.f, p3 = 0.f;
        if (k < cnt) {
          const float a = tr16(dot8(kb[s][0], q), dot8(kb[s][2], q)), b = tr16(dot8(kb[s][1], q), dot8(kb[s][3], q));
          const float t1 = a + dpp<XOR8>(a), t2 = b + dpp<XOR8>(b);
          float y = (lane & 8) ? t2 : t1;
          y += dpp<XOR7>(y); y += dpp<XOR2>(y); y += dpp<XOR1>(y);
          y = rbf(y);
          float mu = fmaxf(y, dpp<XOR8>(y)); mu = max16(mu); mu = max32(mu);
          if (__builtin_amdgcn_readfirstlane(mu > m ? 1 : 0)) {
            const float al = ex2((m - mu) * CEXP);
            l *= al;
#pragma unroll
            for (int e = 0; e < 8; ++e) acc[e] *= al;
            m = mu;
          }
          const float p = ex2((y - m) * CEXP);
          l += p;
          const float px = dpp<XOR8>(p);
          const float pe = (lane & 8) ? px : p, po = (lane & 8) ? p : px;
          auto se = __builtin_amdgcn_permlane16_swap(__float_as_uint(pe), __float_as_uint(pe), false, false);
          auto so2 = __builtin_amdgcn_permlane16_swap(__float_as_uint(po), __float_as_uint(po), false, false);
          p0 = __uint_as_float(se[0]); p2 = __uint_as_float(se[1]); p1 = __uint_as_float(so2[0]); p3 = __uint_as_float(so2[1]);
        }
#pragma unroll
        for (int i = 0; i < 4; ++i) { if ((i & 1) == 0 && more) WAIT_VM_N(PACE_A - 2); kb[s][i] = ld16<AUX_NT>(rk, vo + i * 1024, so); }
        if (k < cnt) {
#pragma unroll
          for (int i = 0; i < 4; ++i) {
            const float pi = i == 0 ? p0 : i == 1 ? p1 : i == 2 ? p2 : p3;
#pragma unroll
            for (int e = 0; e < 4; ++e) {
              acc[2 * e] = fmaf(pi, blo(vb[s][i][e]), acc[2 * e]);
              acc[2 * e + 1] = fmaf(pi, bhi(vb[s][i][e]), acc[2 * e + 1]);
            }
          }
        }
#pragma unroll
        for (int i = 0; i < 4; ++i) { if ((i & 1) == 0 && more) WAIT_VM_N(PACE_A - 2); vb[s][i] = ld16<AUX_NT>(rv, vo + i * 1024, so); }
      }
    }
    STAMP(6);
#pragma unroll
    for (int e = 0; e < 4; ++e) acc[e] = tr32(acc[e], acc[e + 4]);
    l = tr32(l, l); l = tr16(l, l); l += dpp<XOR8>(l);
    float* mrg = (float*)(lds + L_BIG);
    *(f32x4*)(mrg + wave * 260 + 4 + (lane & 31) * 8 + (lane >> 5) * 4) = f32x4{acc[0], acc[1], acc[2], acc[3]};
    if (lane == 0) *(f32x2*)(mrg + wave * 260) = f32x2{m, l};
    BARRIER();
    if (wave < 2) {
      const int t = wave * 64 + lane;
      float M = -INFINITY;
#pragma unroll
      for (int w = 0; w < 8; ++w) M = fmaxf(M, mrg[w * 260]);
      float L = 0.f, o0 = 0.f, o1 = 0.f;
#pragma unroll
      for (int w = 0; w < 8; ++w) {
        const f32x2 ml = *(const f32x2*)(mrg + w * 260), ov = *(const f32x2*)(mrg + w * 260 + 4 + 2 * t);
        const float e = ex2((ml[0] - M) * CEXP);
        L = fmaf(e, ml[1], L); o0 = fmaf(e, ov[0], o0); o1 = fmaf(e, ov[1], o1);
      }
      const u32 rec = (hA * 2 + bA) * 8 + ja;
      st16(v4u{genc(o0, tg), tg.hi, genc(o1, tg), tg.hi}, rws, WS_OA + rec * 2048 + t * 16);
      if (t == 0) st16(v4u{genc(M, tg), tg.hi, genc(L, tg), tg.hi}, rws, WS_SA + rec * 16);
    }
    STAMP(7);
  } else if (roleC) {
    const int lane = get_lane();
    const int ci = xc * 4 + r, bC = ci & 1, sC = ci >> 1;
    const rsrc_t rc = mkrsrc(Bc + ((size_t)bC * 4096 + sC * 256) * 1024, 256u * 2048u);
    const u32 vo = wave * 8192 + lane * 16;
    v4u buf[NSLOT_C][8];
    bool ready = false;
    const int g4 = lane >> 4, r16 = lane & 15;
    const int pg = 2 * (g4 & 1) + (g4 >> 1);
    const int ktq = wave & 1, cq = wave >> 1;
    const u32 tl = (u32)T, th = (u32)(T >> 32);
    v4u fl = {0u, 0u, 0u, 0u};
    const u32 pollv = wave < 2 ? WS_F2 + wave * 1024 + lane * 16 : OOB;
    const u32 wbase0 = g4 * 8192 + r16 * 16;
#define IMG_ADDR(row_, half_) ((wbase ^ (((((row_) & 7) << 1) | (((row_) >> 3) & 1)) * 16)) + (row_) * 256 + (half_) * 32768)
#define WRITE_IMG(rd_) do { char* img_ = lds + L_BIG; u32 wbase = wbase0; asm volatile("" : "+v"(wbase)); \
      _Pragma("unroll") for (int i_ = 0; i_ < 8; ++i_)     \
        *(v4u*)(img_ + IMG_ADDR(4 * wave + (i_ >> 1), i_ & 1)) = buf[(rd_) % NSLOT_C][i_]; } while (0)
#define FLAGS_BAD() ((fl[0] ^ tl) | (fl[1] ^ th) | (fl[2] ^ tl) | (fl[3] ^ th))
#define BOTH_SET() ((__builtin_amdgcn_readfirstlane(ctl[L_QAF / 4]) & __builtin_amdgcn_readfirstlane(ctl[L_QAF / 4 + 1])) != 0)
#define STAGE_Q() do { CFENCE(); v4u t_[4]; u32 wbase = wbase0; asm volatile("" : "+v"(wbase)); \
      _Pragma("unroll") for (int i_ = 0; i_ < 4; ++i_) t_[i_] = ld16<AUX_POLL>(rws, WS_QABS + bC * 32768 + wave * 4096 + lane * 16 + i_ * 1024, 0); \
      _Pragma("unroll") for (int i_ = 0; i_ < 4; ++i_) *(v4u*)(lds + L_XQ + IMG_ADDR(2 * wave + (i_ >> 1), i_ & 1)) = t_[i_]; \
      ready = true; } while (0)
#pragma unroll
    for (int rd = 0; rd < NSLOT_C + 1; ++rd) {
      if (rd == NSLOT_C) WRITE_IMG(0);
#pragma unroll
      for (int i = 0; i < 8; ++i) {
        if ((i & 1) == 0) {
          WAIT_VM_N(PACE_C - 2);
          if (rd >= 1) {
            if ((i & 3) == 0) {
              if (wave < 2 && all0(FLAGS_BAD())) ctl[L_QAF / 4 + wave] = 1;
              CFENCE();
              fl = ld16<AUX_POLL>(rws, pollv, 0);
            }
            if (!ready && BOTH_SET()) STAGE_Q();
          }
        }
        buf[rd % NSLOT_C][i] = ld16<AUX_NT>(rc, vo + (i & 3) * 1024, rd * 65536 + (i >> 2) * 4096);
      }
    }
    STAMP(4);
    if (!ready) {
      if (wave < 2) {
#pragma unroll 1
        for (u32 spin = 0; spin < SPIN_CAP; ++spin) {
          if (all0(FLAGS_BAD())) break;
          __builtin_amdgcn_s_sleep(1);
          CFENCE();
          fl = ld16<AUX_POLL>(rws, pollv, 0);
        }
        ctl[L_QAF / 4 + wave] = 1;
      }
      lds_wait(ctl + L_QAF / 4, 1u); lds_wait(ctl + L_QAF / 4 + 1, 1u);
      STAGE_Q();
    }
    STAMP(5);
    const u32 fr = ((r16 & 7) << 1) | (r16 >> 3);
    const u32 qaddr = cq * 16384 + r16 * 256 + ((pg ^ fr) * 16);
    const u32 raddr0 = qaddr + ktq * 4096;
    const int trow = 4 * g4 + ((lane >> 2) & 3);
    const u32 ft = ((trow & 7) << 1) | (trow >> 3);
    const u32 taddr0 = wave * 8192 + trow * 256 + ((((lane >> 1) & 1) ^ ft) * 16) + (lane & 1) * 8;
    f32x4 acc[8];
#pragma unroll
    for (int t = 0; t < 8; ++t) acc[t] = f32x4{0.f, 0.f, 0.f, 0.f};
    float m = -INFINITY, l = 0.f;
    v4u qf[8];
#pragma unroll
    for (int rd = 0; rd < 8; ++rd) {
      char* img = lds + L_BIG;
      if (rd >= 1) {
        WRITE_IMG(rd);
        if (rd + NSLOT_C < 8) {
#pragma unroll
          for (int i = 0; i < 8; ++i) buf[rd % NSLOT_C][i] = ld16<AUX_NT>(rc, vo + (i & 3) * 1024, (rd + NSLOT_C) * 65536 + (i >> 2) * 4096);
        }
      }
      BARRIER();
      if (rd == 0) {
#pragma unroll
        for (int ks = 0; ks < 8; ++ks) qf[ks] = *(const v4u*)(lds + L_XQ + (qaddr ^ ((ks & 3) * 64)) + (ks >> 2) * 8192);
      }
      f32x4 sa = {0.f, 0.f, 0.f, 0.f}, sb = sa;
      u32 raddr = raddr0, taddr = taddr0; asm volatile("" : "+v"(raddr), "+v"(taddr));
#pragma unroll
      for (int ks = 0; ks < 4; ++ks) {
        const v4u a = *(const v4u*)(img + (raddr ^ (ks * 64))), b = *(const v4u*)(img + (raddr ^ (ks * 64)) + 8192);
        sa = __builtin_amdgcn_mfma_f32_16x16x32_bf16(__builtin_bit_cast(bf16x8, a), __builtin_bit_cast(bf16x8, qf[ks]), sa, 0, 0, 0);
        sb = __builtin_amdgcn_mfma_f32_16x16x32_bf16(__builtin_bit_cast(bf16x8, b), __builtin_bit_cast(bf16x8, qf[ks + 4]), sb, 0, 0, 0);
      }
      __builtin_amdgcn_sched_barrier(0);
      v4u af[8];
#pragma unroll
      for (int t = 0; t < 8; ++t) {
        const s16x4 a0 = __builtin_amdgcn_ds_read_tr16_b64_v4i16((LAS s16x4*)(img + (taddr ^ (t * 32))));
        const s16x4 a1 = __builtin_amdgcn_ds_read_tr16_b64_v4i16((LAS s16x4*)(img + (taddr ^ (t * 32)) + 4096));
        const v2u u0 = __builtin_bit_cast(v2u, a0), u1 = __builtin_bit_cast(v2u, a1);
        af[t] = v4u{u0[0], u0[1], u1[0], u1[1]};
      }
      char* X = lds + L_XQ + 4096 + lane * 16;
      *(f32x4*)(X + wave * 8192) = sa + sb;
      BARRIER();
      f32x4 sc[2];
      sc[0] = (*(const f32x4*)(X) + *(const f32x4*)(X + 16384)) + (*(const f32x4*)(X + 32768) + *(const f32x4*)(X + 49152));
      __builtin_amdgcn_sched_barrier(0);
      sc[1] = (*(const f32x4*)(X + 8192) + *(const f32x4*)(X + 24576)) + (*(const f32x4*)(X + 40960) + *(const f32x4*)(X + 57344));
      float mu = fmaxf(fmaxf(fmaxf(sc[0][0], sc[0][1]), fmaxf(sc[0][2], sc[0][3])), fmaxf(fmaxf(sc[1][0], sc[1][1]), fmaxf(sc[1][2], sc[1][3])));
      mu = max16(mu); mu = max32(mu);
      const float mn = fmaxf(m, mu);
      const float al = ex2((m - mn) * CEXP);
      m = mn;
      float p[8];
#pragma unroll
      for (int e = 0; e < 8; ++e) p[e] = ex2((sc[e >> 2][e & 3] - mn) * CEXP);
      l = fmaf(l, al, ((p[0] + p[1]) + (p[2] + p[3])) + ((p[4] + p[5]) + (p[6] + p[7])));
      const v4u pf = {pkbf(p[0], p[1]), pkbf(p[2], p[3]), pkbf(p[4], p[5]), pkbf(p[6], p[7])};
#pragma unroll
      for (int t = 0; t < 8; ++t) {
        acc[t] *= al;
        acc[t] = __builtin_amdgcn_mfma_f32_16x16x32_bf16(__builtin_bit_cast(bf16x8, af[t]), __builtin_bit_cast(bf16x8, pf), acc[t], 0, 0, 0);
      }
#pragma unroll
      for (int t = 0; t < 8; ++t) asm volatile("" : "+v"(acc[t]));
      if (rd < 5) STAMP(11 + rd);
    }
    STAMP(6);
    l = tr16(l, l); l = tr32(l, l);
    const float inv = 1.f / l;
    const u32 dst = WS_P3 + ((bC * 16 + sC) * 16 + r16) * 2048 + wave * 256 + g4 * 16;
#pragma unroll
    for (int tp = 0; tp < 4; ++tp) {
      const f32x4 a = acc[2 * tp] * inv, b = acc[2 * tp + 1] * inv;
      st16(v4u{pkbf(a[0], a[1]), pkbf(a[2], a[3]), pkbf(b[0], b[1]), pkbf(b[2], b[3])}, rws, dst + tp * 64);
    }
    st8(v2u{__float_as_uint(m), __float_as_uint(l)}, rws, (wave == 0 && lane < 16) ? WS_ST3 + ((bC * 16 + lane) * 16 + sC) * 8 : OOB);
    WAIT_VM(0);
    BARRIER();
    if (wave == 0) st8(v2u{(u32)T, (u32)(T >> 32)}, rws, lane == 0 ? WS_F3 + ci * 8 : OOB);
    STAMP(7);
    const int hP = ci >> 1, dh = ci & 1;
    v4u w[16];
    {
      const rsrc_t rw = mkrsrc(Awo + (size_t)(hP * 256 + dh * 128 + wave * 16) * 448, 16u * 896u);
      const u32 wo = lane < 56 ? lane * 16 : OOB;
#pragma unroll
      for (int i = 0; i < 16; ++i) w[i] = ld16<AUX_NT>(rw, wo, i * 896);
    }
    pair_rows(w);
    STAMP(8);
    const int jq = lane >> 4, bq = (lane >> 3) & 1, pr = lane & 7;
    const bool two = jq < 2;
    const u32 rec = (hP * 2 + bq) * 8 + jq;
    const u32 oo = WS_OA + rec * 2048 + (dh * 128 + wave * 16 + 2 * pr) * 8, os = WS_SA + rec * 16;
    const u32 oo2 = two ? oo + 4 * 2048 : oo, os2 = two ? os + 4 * 16 : os;
    v4u a0, a1, s0, s1;
#pragma unroll 1
    for (u32 spin = 0; spin < SPIN_CAP; ++spin) {
      CFENCE();
      a0 = ld16<AUX_POLL>(rws, oo, 0); a1 = ld16<AUX_POLL>(rws, oo2, 0); s0 = ld16<AUX_POLL>(rws, os, 0); s1 = ld16<AUX_POLL>(rws, os2, 0);
      if (all0(gbad(a0, tg) | gbad(a1, tg) | gbad(s0, tg) | gbad(s1, tg))) break;
      __builtin_amdgcn_s_sleep(1);
    }
    STAMP(9);
    const float m0 = gdec(s0[0]), m1 = gdec(s1[0]);
    float M = fmaxf(m0, m1); M = max16(M); M = max32(M);
    const float e0 = ex2((m0 - M) * CEXP), e1 = two ? ex2((m1 - M) * CEXP) : 0.f;
    float L = e0 * gdec(s0[2]) + e1 * gdec(s1[2]);
    float o0 = e0 * gdec(a0[0]) + e1 * gdec(a1[0]), o1 = e0 * gdec(a0[2]) + e1 * gdec(a1[2]);
    L = tr16(L, L); L = tr32(L, L); o0 = tr16(o0, o0); o0 = tr32(o0, o0); o1 = tr16(o1, o1); o1 = tr32(o1, o1);
    const float il = 1.f / L;
    const u32 opk = pkbf(o0 * il, o1 * il);
    u32 op[16];
#pragma unroll
    for (int i = 0; i < 16; ++i) op[i] = __builtin_amdgcn_readlane(opk, i);
    project_publish(w, op, lds, rws, WS_OUTP + ci * 7168, tg, wave, lane);
    STAMP(10);
  } else {
    const int lane0 = get_lane();
    const int wi = xc * 4 + (r - 4), hW = wi >> 1, dh = wi & 1;
    v4u uv[32], w[16];
    {
      const rsrc_t ru = mkrsrc(Bwuv, 1024u * 8192u);
      const u32 vo = ((u32)(wave * 128 + 32 * (lane0 >> 4)) * 16 + hW) * 512u + dh * 256 + (lane0 & 15) * 16;
#pragma unroll
      for (int i = 0; i < 32; ++i) { if ((i & 1) == 0) WAIT_VM_N(PACE_W - 2); uv[i] = ld16<AUX_NT>(ru, vo, i * 8192); }
      const rsrc_t rw = mkrsrc(Bwo + (size_t)(hW * 256 + dh * 128 + wave * 16) * 448, 16u * 896u);
      const u32 wo = lane0 < 56 ? lane0 * 16 : OOB;
#pragma unroll
      for (int i = 0; i < 16; ++i) { if ((i & 1) == 0) WAIT_VM_N(PACE_W - 2); w[i] = ld16<AUX_NT>(rw, wo, i * 896); }
    }
    pair_rows(*(v4u(*)[16])&uv[0]); pair_rows(*(v4u(*)[16])&uv[16]);
    pair_rows(w);
    STAMP(4);
    const int lane = get_lane();
    const int g4 = lane >> 4, r16 = lane & 15;
#pragma unroll 1
    for (u32 spin = 0; spin < SPIN_CAP; ++spin) {
      CFENCE();
      const v2u f = ld8<AUX_POLL>(rws, WS_F3 + (lane & 31) * 8, 0);
      if (all0((f[0] ^ (u32)T) | (f[1] ^ (u32)(T >> 32)))) break;
      __builtin_amdgcn_s_sleep(4);
    }
    asm volatile("" ::: "memory");
    STAMP(5);
    if (wi == 0 && wave == 0) {
      const u64 E = (((u64)ctl[7] << 32) | ctl[6]) + 1ull;
      st8(v2u{(u32)E, (u32)(E >> 32)}, rws, lane == 0 ? WS_E : OOB);
    }
    const int bq = g4 >> 1, sp = g4 & 1;
    float cc[8];
    {
      const v4u sa = ld16<AUX_POLL>(rws, WS_ST3 + ((bq * 16 + hW) * 16 + (r16 & 7) * 2) * 8, 0);
      v4u pa[8];
      const u32 po = WS_P3 + ((bq * 16 + sp) * 16 + hW) * 2048 + wave * 256 + r16 * 16;
#pragma unroll
      for (int i = 0; i < 8; ++i) pa[i] = ld16<AUX_POLL>(rws, po, i * 65536);
      const float ma = __uint_as_float(sa[0]), mb = __uint_as_float(sa[2]);
      float M = fmaxf(ma, mb);
      M = fmaxf(M, dpp<XOR1>(M)); M = fmaxf(M, dpp<XOR2>(M)); M = fmaxf(M, dpp<XOR7>(M));
      const float ea = ex2((ma - M) * CEXP) * __uint_as_float(sa[1]), eb = ex2((mb - M) * CEXP) * __uint_as_float(sa[3]);
      float L = ea + eb;
      L += dpp<XOR1>(L); L += dpp<XOR2>(L); L += dpp<XOR7>(L);
      const float wsel = (sp ? eb : ea) / L;
#pragma unroll
      for (int e = 0; e < 8; ++e) cc[e] = 0.f;
#pragma unroll
      for (int i = 0; i < 8; ++i) {
        const float wt = __shfl(wsel, (lane & 48) + i);
#pragma unroll
        for (int e = 0; e < 4; ++e) { cc[2 * e] = fmaf(wt, blo(pa[i][e]), cc[2 * e]); cc[2 * e + 1] = fmaf(wt, bhi(pa[i][e]), cc[2 * e + 1]); }
      }
#pragma unroll
      for (int e = 0; e < 4; ++e) cc[e] = tr16(cc[e], cc[e + 4]);
    }
    u32* cl = (u32*)(lds + L_BIG + wave * 512);
    *(v2u*)(cl + bq * 64 + 16 * (r16 >> 2) + 8 * sp + 2 * (r16 & 3)) = v2u{pkbf(cc[0], cc[1]), pkbf(cc[2], cc[3])};
    WAIT_LGKM();
    STAMP(6);
    float v[16];
#pragma unroll
    for (int k = 0; k < 16; ++k) v[k] = 0.f;
#pragma unroll
    for (int i4 = 0; i4 < 4; ++i4) {
      const v4u ca = *(const v4u*)(cl + 16 * g4 + 4 * i4), cb2 = *(const v4u*)(cl + 64 + 16 * g4 + 4 * i4);
#pragma unroll
      for (int u = 0; u < 4; ++u)
#pragma unroll
        for (int k = 0; k < 4; ++k) {
          const u32 w0 = uv[8 * i4 + 2 * u][k], w1 = uv[8 * i4 + 2 * u + 1][k];
          v[2 * k] = dot2(w0, ca[u], v[2 * k]); v[2 * k + 1] = dot2(w1, ca[u], v[2 * k + 1]);
          v[8 + 2 * k] = dot2(w0, cb2[u], v[8 + 2 * k]); v[8 + 2 * k + 1] = dot2(w1, cb2[u], v[8 + 2 * k + 1]);
        }
    }
#pragma unroll
    for (int k = 0; k < 8; ++k) v[k] = tr32(v[k], v[k + 8]);
#pragma unroll
    for (int k = 0; k < 4; ++k) v[k] = tr16(v[k], v[k + 4]);
    float* cx = (float*)(lds + L_BIG + 8192);
    *(f32x4*)(cx + wave * 256 + (lane >> 5) * 128 + r16 * 8 + (g4 & 1) * 4) = f32x4{v[0], v[1], v[2], v[3]};
    BARRIER();
    u32 opk;
    {
      const int b2 = (lane >> 3) & 1, pr = lane & 7;
      float c0 = 0.f, c1 = 0.f;
#pragma unroll
      for (int w8 = 0; w8 < 8; ++w8) { const f32x2 t = *(const f32x2*)(cx + w8 * 256 + b2 * 128 + wave * 16 + 2 * pr); c0 += t[0]; c1 += t[1]; }
      opk = pkbf(c0, c1);
    }
    u32 op[16];
#pragma unroll
    for (int i = 0; i < 16; ++i) op[i] = __builtin_amdgcn_readlane(opk, i);
    project_publish(w, op, lds, rws, WS_OUTP + (32 + wi) * 7168, tg, wave, lane);
    STAMP(7);
    if (wave < 7) {
      final_reduce(rws, WS_OUTP + 32 * 7168, tg, out + 448, wi * 7 + wave, lane);
      STAMP(8);
      final_reduce(rws, WS_OUTP, tg, out, wi * 7 + wave, lane);
      STAMP(9);
    }
  }
}

extern "C" void kernel_launch(void* const* d_in, const int* in_sizes, int n_in, void* d_out, int out_size, void* d_ws,
                              size_t ws_size, hipStream_t stream) {
  fused_decode<<<256, 512, 0, stream>>>((const u16*)d_in[0], (const u16*)d_in[1], (const u16*)d_in[2], (const u16*)d_in[3],
                                        (const u16*)d_in[4], (const u16*)d_in[5], (const u16*)d_in[6], (const u16*)d_in[7],
                                        (const u16*)d_in[8], (const u16*)d_in[9], (const u16*)d_in[10], (u16*)d_out, (char*)d_ws);
}
```

```cpp
#include <hip/hip_runtime.h>
#include <stdint.h>

typedef unsigned short u16;
typedef unsigned int u32;
typedef unsigned long long u64;
typedef u32 v4u __attribute__((ext_vector_type(4)));
typedef u32 v2u __attribute__((ext_vector_type(2)));
typedef float f32x4 __attribute__((ext_vector_type(4)));
typedef float f32x2 __attribute__((ext_vector_type(2)));
typedef __bf16 bf16x2 __attribute__((ext_vector_type(2)));
typedef __bf16 bf16x8 __attribute__((ext_vector_type(8)));
typedef short s16x4 __attribute__((ext_vector_type(4)));
typedef __amdgpu_buffer_rsrc_t rsrc_t;
#define LAS __attribute__((address_space(3)))

constexpr int PACE_A0 = 4;
constexpr int PACE_A = 6, PACE_C = 6, PACE_W = 4;
constexpr int POLL_SLEEP = 8;
constexpr int EARLY = 4;
constexpr int UNITS_W0 = 9;
constexpr int UNITS_C1 = 32;

constexpr u32 WS_E = 0, WS_ST3 = 16384  , WS_QG = 32768, WS_QABS = 163840, WS_F2 = 229376  ,
              WS_OA = 262144  , WS_OUTP = 1048576, WS_P3 = 2097152, WS_BYTES = 4194304;
constexpr u32 OA_REC = 2176;
constexpr int L_T = 0, L_PUB = 12, L_QAF = 16, L_GO = 20, L_E = 24, L_CNT1 = 32, L_F2 = 64;
constexpr int L_S1P = 256;
constexpr int L_QA = 2304;
constexpr int L_QB = 2816;
constexpr int L_O = 6144;
constexpr int L_BIG = 8192;
constexpr int L_XQ = L_BIG + 65536;
constexpr int LDS_BYTES = L_XQ + 65536;
constexpr u32 SPIN_CAP = 200000;
constexpr float CEXP = 0.0625f * 1.4426950408889634f;
constexpr u32 OOB = 0x40000000u;
constexpr int AUX_NT = 2, AUX_SC1 = 16;
constexpr int AUX_UK = 0, AUX_CK = 0, AUX_UV = 0, AUX_BO = 0, AUX_AO = 0;

#define CFENCE() asm volatile("" ::: "memory")
#define WAIT_VM(n) asm volatile("s_waitcnt vmcnt(%0)" :: "i"(n) : "memory")
#define WAIT_LGKM() asm volatile("s_waitcnt lgkmcnt(0)" ::: "memory")
#define BARRIER() do { WAIT_LGKM(); __builtin_amdgcn_s_barrier(); CFENCE(); } while (0)

__device__ __forceinline__ rsrc_t mkrsrc(const void* p, u32 bytes) {
  return __builtin_amdgcn_make_buffer_rsrc(const_cast<void*>(p), 0, bytes, 0x00020000);
}
template <int AUX> __device__ __forceinline__ v4u ld16(rsrc_t r, u32 voff, u32 soff) {
  return __builtin_amdgcn_raw_buffer_load_b128(r, voff, soff, AUX);
}
template <int AUX> __device__ __forceinline__ v2u ld8(rsrc_t r, u32 voff, u32 soff) {
  return __builtin_amdgcn_raw_buffer_load_b64(r, voff, soff, AUX);
}
__device__ __forceinline__ void st16(v4u v, rsrc_t r, u32 voff) { __builtin_amdgcn_raw_buffer_store_b128(v, r, voff, 0, AUX_SC1); }
__device__ __forceinline__ void st8(v2u v, rsrc_t r, u32 voff) { __builtin_amdgcn_raw_buffer_store_b64(v, r, voff, 0, AUX_SC1); }
__device__ __forceinline__ void st4(u32 v, rsrc_t r, u32 voff) { __builtin_amdgcn_raw_buffer_store_b32(v, r, voff, 0, AUX_SC1); }

__device__ __forceinline__ float dot2(u32 k, u32 q, float acc) {
  return __builtin_amdgcn_fdot2_f32_bf16(__builtin_bit_cast(bf16x2, k), __builtin_bit_cast(bf16x2, q), acc, false);
}
__device__ __forceinline__ float dot8(v4u k, v4u q) {
  float d = dot2(k[0], q[0], 0.f); d = dot2(k[1], q[1], d); d = dot2(k[2], q[2], d); return dot2(k[3], q[3], d);
}
__device__ __forceinline__ u32 pkbf(float lo, float hi) {
  f32x2 x = {lo, hi};
  return __builtin_bit_cast(u32, __builtin_convertvector(x, bf16x2));
}
__device__ __forceinline__ float rbf(float f) { return __uint_as_float(pkbf(f, f) & 0xffff0000u); }
__device__ __forceinline__ float blo(u32 v) { return __uint_as_float(v << 16); }
__device__ __forceinline__ float bhi(u32 v) { return __uint_as_float(v & 0xffff0000u); }
template <int CTRL> __device__ __forceinline__ float dpp(float x) {
  return __builtin_bit_cast(float, __builtin_amdgcn_mov_dpp(__builtin_bit_cast(int, x), CTRL, 0xf, 0xf, true));
}
constexpr int XOR1 = 0xB1, XOR2 = 0x4E, XOR7 = 0x141, XOR8 = 0x128;
__device__ __forceinline__ float xor4(float x) {
  int v = __builtin_bit_cast(int, x);
  int t = __builtin_amdgcn_update_dpp(0, v, 0x114, 0xf, 0xa, false);
  t = __builtin_amdgcn_update_dpp(t, v, 0x104, 0xf, 0x5, false);
  return __builtin_bit_cast(float, t);
}
__device__ __forceinline__ float tr16(float a, float b) {
  auto s = __builtin_amdgcn_permlane16_swap(__float_as_uint(a), __float_as_uint(b), false, false);
  return __uint_as_float(s[0]) + __uint_as_float(s[1]);
}
__device__ __forceinline__ float tr32(float a, float b) {
  auto s = __builtin_amdgcn_permlane32_swap(__float_as_uint(a), __float_as_uint(b), false, false);
  return __uint_as_float(s[0]) + __uint_as_float(s[1]);
}
__device__ __forceinline__ float max16(float a) {
  auto s = __builtin_amdgcn_permlane16_swap(__float_as_uint(a), __float_as_uint(a), false, false);
  return fmaxf(__uint_as_float(s[0]), __uint_as_float(s[1]));
}
__device__ __forceinline__ float max32(float a) {
  auto s = __builtin_amdgcn_permlane32_swap(__float_as_uint(a), __float_as_uint(a), false, false);
  return fmaxf(__uint_as_float(s[0]), __uint_as_float(s[1]));
}
__device__ __forceinline__ float ex2(float x) { return __builtin_amdgcn_exp2f(x); }
__device__ __forceinline__ f32x4 mfma(v4u a, v4u b, f32x4 c) {
  return __builtin_amdgcn_mfma_f32_16x16x32_bf16(__builtin_bit_cast(bf16x8, a), __builtin_bit_cast(bf16x8, b), c, 0, 0, 0);
}

struct Tag { u32 lo, hi; };
__device__ __forceinline__ u32 genc(float f, Tag t) { return ((__float_as_uint(f) + 0x80u) >> 8) | t.lo; }
__device__ __forceinline__ float gdec(u32 lo) { return __uint_as_float(lo << 8); }
__device__ __forceinline__ u32 gbad(v4u g, Tag t) { return (g[1] ^ t.hi) | (g[3] ^ t.hi) | ((g[0] ^ t.lo) >> 24) | ((g[2] ^ t.lo) >> 24); }
__device__ __forceinline__ u32 gq(v4u g) { return ((g[0] >> 8) & 0xffffu) | ((g[2] << 8) & 0xffff0000u); }
__device__ __forceinline__ bool all0(u32 x) { return __builtin_amdgcn_ballot_w64(x != 0u) == 0ull; }
__device__ __forceinline__ int get_lane() {
  int l; asm volatile("v_mbcnt_lo_u32_b32 %0, -1, 0\n\tv_mbcnt_hi_u32_b32 %0, -1, %0" : "=v"(l)); return l;
}
__device__ __forceinline__ void lds_wait(volatile LAS u32* p, u32 target) {
#pragma unroll 1
  for (u32 spin = 0; spin < SPIN_CAP * 8u; ++spin) {
    if ((u32)__builtin_amdgcn_readfirstlane(*p) >= target) break;
    __builtin_amdgcn_s_sleep(1);
  }
  CFENCE();
}

__device__ __forceinline__ void to_frags(v4u* r) {
  v4u f[8];
#pragma unroll
  for (int k = 0; k < 4; ++k)
#pragma unroll
    for (int jp = 0; jp < 4; ++jp) {
      f[2 * k][jp] = __builtin_amdgcn_perm(r[2 * jp + 1][k], r[2 * jp][k], 0x05040100u);
      f[2 * k + 1][jp] = __builtin_amdgcn_perm(r[2 * jp + 1][k], r[2 * jp][k], 0x07060302u);
    }
#pragma unroll
  for (int e = 0; e < 8; ++e) { r[e] = f[e]; asm volatile("" : "+v"(r[e])); }
}
template <int PACE, int AUX> __device__ __forceinline__ void load_proj(v4u (&w)[16], const u16* base, int wave, int lane) {
  const rsrc_t rw = mkrsrc(base, wave < 7 ? 128u * 896u : 0u);
  const u32 vo = (64 * ((lane >> 3) & 1) + 8 * (lane >> 4)) * 896 + (8 * wave + (lane & 7)) * 16;
#pragma unroll
  for (int i = 0; i < 16; ++i) { if ((i & 1) == 0) WAIT_VM(PACE - 2); w[i] = ld16<AUX>(rw, vo, (32 * (i >> 3) + (i & 7)) * 896); }
}
__device__ __forceinline__ void project_publish(const v4u (&w)[16], char* lds, rsrc_t rws, u32 dst, Tag tg, int wave, int lane) {
  if (wave >= 7) return;
  const char* oa = lds + L_O + (lane & 1) * 256 + ((lane >> 1) & 1) * 128 + (lane >> 4) * 16;
  const v4u o0 = *(const v4u*)oa, o1 = *(const v4u*)(oa + 64);
  f32x4 d[8];
#pragma unroll
  for (int e = 0; e < 8; ++e) { d[e] = mfma(o0, w[e], f32x4{0.f, 0.f, 0.f, 0.f}); }
#pragma unroll
  for (int e = 0; e < 8; ++e) d[e] = mfma(o1, w[8 + e], d[e]);
  float* tp = (float*)(lds + L_BIG + wave * 512);
#pragma unroll
  for (int e = 0; e < 8; ++e) { d[e][0] += dpp<XOR8>(d[e][2]); d[e][1] += dpp<XOR8>(d[e][3]); }
  CFENCE();
  if (lane < 8) {
#pragma unroll
    for (int e = 0; e < 8; e += 2) *(f32x4*)(tp + lane * 16 + e * 2) = f32x4{d[e][0], d[e][1], d[e + 1][0], d[e + 1][1]};
  }
  WAIT_LGKM();
  const f32x2 v = *(const f32x2*)(tp + lane * 2);
  st16(v4u{genc(v[0], tg), tg.hi, genc(v[1], tg), tg.hi}, rws, dst + (wave * 64 + lane) * 16);
}

__global__ void __launch_bounds__(512, 2)
fused_decode(const u16* __restrict__ Ax, const u16* __restrict__ Ak, const u16* __restrict__ Av, const u16* __restrict__ Awq,
             const u16* __restrict__ Awo, const u16* __restrict__ Bx, const u16* __restrict__ Bc, const u16* __restrict__ Bwq,
             const u16* __restrict__ Bwuk, const u16* __restrict__ Bwuv, const u16* __restrict__ Bwo, u16* out, char* wsp) {
  __shared__ __attribute__((aligned(16))) char lds[LDS_BYTES];
  const int lane = threadIdx.x & 63, wave = __builtin_amdgcn_readfirstlane(threadIdx.x >> 6);
  const int g = blockIdx.x, xc = g & 7, r = g >> 3;
  const rsrc_t rws = mkrsrc(wsp, WS_BYTES);
  volatile LAS u32* ctl = (volatile LAS u32*)lds;

  const int cb = xc * 32 + r, mat = cb >> 7, cbm = cb & 127;
  const bool roleC = r < 8, roleW = (r >= 8) && (r < 12);
  const bool s2wave = roleC ? false : (roleW ? wave < 3 : wave == 0);
  const int t2 = roleW ? 20 + 3 * (r - 8) + wave : r - 12;
  const int h2 = 2 * xc + (t2 >> 4), cs2 = t2 & 15;
  Tag tg; u64 T = 0;
  v4u wk[32];
  const u32 vuk = ((u32)(cs2 * 64 + 32 * (lane >> 5)) * 16 + h2) * 512u + (lane & 31) * 16;
  const int ci = xc * 8 + r, bC = ci & 1, sC = ci >> 1;
  const u16* cbase = Bc + ((size_t)bC * 4096 + sC * 128) * 1024;
  if (wave > 0) {
    v4u w1[4]; v2u x0, x1;
    const rsrc_t rw = mkrsrc(mat ? Bwq : Awq, 448u * 8192u);
    const rsrc_t rx = mkrsrc(mat ? Bx : Ax, 2u * 896u);
    const u32 row0 = 64 * (wave - 1) + 4 * (lane >> 2);
    const u32 vo = row0 * 8192u + cbm * 64 + (lane & 3) * 16;
#pragma unroll
    for (int i = 0; i < 4; ++i) w1[i] = ld16<0>(rw, vo, i * 8192);
    x0 = ld8<0>(rx, row0 * 2, 0); x1 = ld8<0>(rx, row0 * 2, 896);
    BARRIER();
    float v[16];
#pragma unroll
    for (int k = 0; k < 16; ++k) v[k] = 0.f;
#pragma unroll
    for (int i = 0; i < 4; ++i) {
      const float xa = (i & 1) ? bhi(x0[i >> 1]) : blo(x0[i >> 1]);
      const float xb = (i & 1) ? bhi(x1[i >> 1]) : blo(x1[i >> 1]);
#pragma unroll
      for (int k = 0; k < 4; ++k) {
        const float lo = blo(w1[i][k]), hi = bhi(w1[i][k]);
        v[2 * k] = fmaf(xa, lo, v[2 * k]); v[2 * k + 1] = fmaf(xa, hi, v[2 * k + 1]);
        v[8 + 2 * k] = fmaf(xb, lo, v[8 + 2 * k]); v[8 + 2 * k + 1] = fmaf(xb, hi, v[8 + 2 * k + 1]);
      }
    }
#pragma unroll
    for (int k = 0; k < 8; ++k) v[k] = tr32(v[k], v[k + 8]);
#pragma unroll
    for (int k = 0; k < 4; ++k) v[k] = tr16(v[k], v[k + 4]);
#pragma unroll
    for (int k = 0; k < 2; ++k) { const float a = v[k] + dpp<XOR8>(v[k]), b = v[k + 2] + dpp<XOR8>(v[k + 2]); v[k] = (lane & 8) ? b : a; }
    { const float a = v[0] + xor4(v[0]), b = v[1] + xor4(v[1]); v[0] = (lane & 4) ? b : a; }
    const int col = (lane & 3) * 8 + ((lane >> 4) & 1) * 4 + ((lane >> 3) & 1) * 2 + ((lane >> 2) & 1);
    ((float*)(lds + L_S1P))[(wave - 1) * 64 + (lane >> 5) * 32 + col] = v[0];
    WAIT_LGKM();
    ctl[L_CNT1 / 4 + wave] = 1;
    if (s2wave) {
      lds_wait(ctl + L_PUB / 4, 1u);
      T = ((u64)ctl[L_T / 4 + 1] << 32) | ctl[L_T / 4];
      const rsrc_t ruk = mkrsrc(Bwuk, 1024u * 8192u);
#pragma unroll
      for (int i = 0; i < 32; ++i) wk[i] = ld16<AUX_UK>(ruk, vuk, i * 8192);
    }
  } else {
    const v2u ev = ld8<AUX_SC1>(rws, WS_E, 0);
    ctl[lane & 31] = (lane & 31) == L_CNT1 / 4 ? 1u : 0u;
    BARRIER();
    const rsrc_t ruk0 = mkrsrc(roleC ? cbase : Bwuk, roleC ? 128u * 2048u : 1024u * 8192u);
    const u32 vuk0 = roleC ? lane * 16 : vuk;
#pragma unroll
    for (int i = 0; i < 32; ++i) wk[i] = ld16<AUX_UK>(ruk0, vuk0, i * 8192);
    __builtin_amdgcn_sched_barrier(0);
    const u64 E = ((u64)__builtin_amdgcn_readfirstlane(ev[1]) << 32) | (u32)__builtin_amdgcn_readfirstlane(ev[0]);
    T = (E + 1ull) * 0x9E3779B97F4A7C15ull + 0xD1B54A32D192ED03ull;
#pragma unroll 1
    for (u32 spin = 0; spin < SPIN_CAP * 8u; ++spin) {
      if (all0(ctl[L_CNT1 / 4 + (lane & 7)] ^ 1u)) break;
      __builtin_amdgcn_s_sleep(1);
    }
    CFENCE();
    float q = 0.f;
#pragma unroll
    for (int w = 0; w < 7; ++w) q += ((const float*)(lds + L_S1P))[w * 64 + lane];
    const u32 lo = (__float_as_uint(rbf(q)) >> 8) | ((u32)(T >> 24) << 24);
    st8(v2u{lo, (u32)(T >> 32)}, rws, WS_QG + ((mat * 2 + (lane >> 5)) * 4096 + cbm * 32 + (lane & 31)) * 8);
    ctl[L_T / 4] = (u32)T; ctl[L_T / 4 + 1] = (u32)(T >> 32); ctl[L_E / 4] = (u32)E; ctl[L_E / 4 + 1] = (u32)(E >> 32);
    WAIT_LGKM();
    ctl[L_PUB / 4] = 1;
  }
  T = ((u64)__builtin_amdgcn_readfirstlane((u32)(T >> 32)) << 32) | (u32)__builtin_amdgcn_readfirstlane((u32)T);
  tg.lo = (u32)(T >> 24) << 24; tg.hi = (u32)(T >> 32);
  const u32 tl = (u32)T, th = (u32)(T >> 32);
#define GET_T() do { lds_wait(ctl + L_PUB / 4, 1u); tg.hi = __builtin_amdgcn_readfirstlane(ctl[L_T / 4 + 1]); \
                     tg.lo = __builtin_amdgcn_readfirstlane(ctl[L_T / 4]) & 0xff000000u; } while (0)

  const int ai = r - 12, gi = roleC ? (r & 3) : ai / 5, ja = roleC ? 5 : ai - gi * 5;
  const int hA = 2 * xc + (gi >> 1), bA = gi & 1;

  if (s2wave) {
    bool doneA = roleW, doneB = false;
    const u32 offA = WS_QG + (bA * 4096 + hA * 256) * 8 + lane * 16;
    const u32 offB = WS_QG + (2 * 4096 + h2 * 256) * 8 + lane * 16;
    u32* qb = (u32*)(lds + L_QB + wave * 1024);
#pragma unroll 1
    for (u32 spin = 0; spin < SPIN_CAP && !(doneA && doneB); ++spin) {
      v4u a0, a1, b0, b1, b2, b3;
      CFENCE();
      if (!doneA) { a0 = ld16<AUX_SC1>(rws, offA, 0); a1 = ld16<AUX_SC1>(rws, offA, 1024); }
      if (!doneB) { b0 = ld16<AUX_SC1>(rws, offB, 0); b1 = ld16<AUX_SC1>(rws, offB, 1024);
                    b2 = ld16<AUX_SC1>(rws, offB, 32768); b3 = ld16<AUX_SC1>(rws, offB, 32768 + 1024); }
      if (!doneA && all0(gbad(a0, tg) | gbad(a1, tg))) {
        u32* qa = (u32*)(lds + L_QA);
        qa[lane] = gq(a0); qa[64 + lane] = gq(a1);
        WAIT_LGKM();
        ctl[L_QAF / 4] = 1;
        doneA = true;
      }
      if (!doneB && all0(gbad(b0, tg) | gbad(b1, tg) | gbad(b2, tg) | gbad(b3, tg))) {
        qb[lane] = gq(b0); qb[64 + lane] = gq(b1); qb[128 + lane] = gq(b2); qb[192 + lane] = gq(b3);
        doneB = true;
      }
    }
    WAIT_LGKM();
    const v4u q0 = *(const v4u*)(qb + (lane & 31) * 4), q1 = *(const v4u*)(qb + 128 + (lane & 31) * 4);
    float v[32];
#pragma unroll
    for (int i = 0; i < 32; ++i) v[i] = tr16(dot8(wk[i], q0), dot8(wk[i], q1));
#define TR_STEP(HALF, CTRL, BIT) _Pragma("unroll") for (int k = 0; k < (HALF); ++k) { \
      const float a_ = v[k] + dpp<CTRL>(v[k]), b_ = v[k + (HALF)] + dpp<CTRL>(v[k + (HALF)]); v[k] = (lane & (BIT)) ? b_ : a_; }
    TR_STEP(16, XOR8, 8) TR_STEP(8, XOR7, 4) TR_STEP(4, XOR2, 2) TR_STEP(2, XOR1, 1)
#undef TR_STEP
    st4(pkbf(v[0], v[1]), rws, WS_QABS + ((((lane >> 4) & 1) * 16 + h2) * 1024 + cs2 * 64 + 32 * (lane >> 5) + 2 * (lane & 15)) * 2);
    WAIT_VM(0);
    st8(v2u{tl, th}, rws, lane == 0 ? WS_F2 + (h2 * 16 + cs2) * 128 : OOB);
    ctl[L_QAF / 4] = 1;
  }

  if (roleW) {
    const int lane0 = get_lane();
    const int wi = xc * 4 + (r - 8), hW = wi >> 1, dh = wi & 1;
    v4u uv[32], w[16];
    {
      const rsrc_t ru = mkrsrc(Bwuv, 1024u * 8192u);
      const u32 vo = ((u32)(wave * 128 + 8 * (lane0 >> 4)) * 16 + hW) * 512u + dh * 256 + (lane0 & 15) * 16;
#pragma unroll
      for (int i = 0; i < 32; ++i) {
        if (i == EARLY) GET_T();
        if ((i & 1) == 0) WAIT_VM(PACE_W - 2);
        uv[i] = ld16<AUX_UV>(ru, vo, (32 * (i >> 3) + (i & 7)) * 8192);
        if (i >= 12 && (i & 7) == 4) to_frags(&uv[(i >> 3) * 8 - 8]);
      }
      to_frags(&uv[24]);
    }
    load_proj<PACE_W, AUX_BO>(w, Bwo + (size_t)(hW * 256 + dh * 128) * 448, wave, lane0);
    to_frags(&w[0]); to_frags(&w[8]);
    const int lane = get_lane();
    const int g4 = lane >> 4, r16 = lane & 15;
    float* wl = (float*)(lds + L_BIG + 16384);
    if (wave == 0) {
      v4u sa;
#pragma unroll 1
      for (u32 spin = 0; spin < SPIN_CAP; ++spin) {
        CFENCE();
        sa = ld16<AUX_SC1>(rws, WS_ST3 + hW * 1024 + lane * 16, 0);
        if (all0(gbad(sa, tg))) break;
        __builtin_amdgcn_s_sleep(2);
      }
      const float ms = gdec(sa[0]);
      float M = ms;
      M = fmaxf(M, dpp<XOR1>(M)); M = fmaxf(M, dpp<XOR2>(M)); M = fmaxf(M, dpp<XOR7>(M)); M = fmaxf(M, dpp<XOR8>(M)); M = max16(M);
      const float ea = ex2((ms - M) * CEXP) * gdec(sa[2]);
      float L = ea;
      L += dpp<XOR1>(L); L += dpp<XOR2>(L); L += dpp<XOR7>(L); L += dpp<XOR8>(L); L = tr16(L, L);
      wl[(lane & 32) + (lane & 1) * 16 + ((lane & 31) >> 1)] = ea / L;
      WAIT_LGKM();
      ctl[L_GO / 4] = 1;
    } else lds_wait(ctl + L_GO / 4, 1u);
    CFENCE();
    if (wi == 0 && wave == 0) {
      const u64 E = (((u64)ctl[L_E / 4 + 1] << 32) | ctl[L_E / 4]) + 1ull;
      st8(v2u{(u32)E, (u32)(E >> 32)}, rws, lane == 0 ? WS_E : OOB);
    }
    const int bq = g4 >> 1, sp = g4 & 1;
    float cc[8];
    {
      v4u pa[6];
      const u32 po = WS_P3 + ((bq * 32 + sp) * 16 + hW) * 2048 + wave * 256 + r16 * 16;
#pragma unroll
      for (int i = 0; i < 6; ++i) pa[i] = ld16<AUX_SC1>(rws, po, i * 65536);
#pragma unroll
      for (int e = 0; e < 8; ++e) cc[e] = 0.f;
#pragma unroll
      for (int i4 = 0; i4 < 4; ++i4) {
        const f32x4 wt = *(const f32x4*)(wl + g4 * 16 + i4 * 4);
#pragma unroll
        for (int u = 0; u < 4; ++u) {
          const int i = i4 * 4 + u;
#pragma unroll
          for (int e = 0; e < 4; ++e) { cc[2 * e] = fmaf(wt[u], blo(pa[i % 6][e]), cc[2 * e]); cc[2 * e + 1] = fmaf(wt[u], bhi(pa[i % 6][e]), cc[2 * e + 1]); }
          if (i + 6 < 16) pa[i % 6] = ld16<AUX_SC1>(rws, po, (i + 6) * 65536);
        }
      }
#pragma unroll
      for (int e = 0; e < 4; ++e) cc[e] = tr16(cc[e], cc[e + 4]);
    }
    u32* cl = (u32*)(lds + L_BIG + 4096 + wave * 512);
    *(v2u*)(cl + bq * 64 + 16 * (r16 >> 2) + 8 * sp + 2 * (r16 & 3)) = v2u{pkbf(cc[0], cc[1]), pkbf(cc[2], cc[3])};
    WAIT_LGKM();
    {
      f32x4 d[8];
#pragma unroll
      for (int e = 0; e < 8; ++e) d[e] = f32x4{0.f, 0.f, 0.f, 0.f};
#pragma unroll
      for (int ks = 0; ks < 4; ++ks) {
        const v4u ca = *(const v4u*)(cl + (lane & 1) * 64 + 16 * ks + 4 * g4);
#pragma unroll
        for (int e = 0; e < 8; ++e) d[e] = mfma(ca, uv[8 * ks + e], d[e]);
      }
      float* cx = (float*)(lds + L_BIG + 8192);
      if (lane < 16) {
#pragma unroll
        for (int b = 0; b < 2; ++b) {
          *(f32x4*)(cx + wave * 256 + b * 128 + lane * 8) = f32x4{d[0][b], d[1][b], d[2][b], d[3][b]};
          *(f32x4*)(cx + wave * 256 + b * 128 + lane * 8 + 4) = f32x4{d[4][b], d[5][b], d[6][b], d[7][b]};
        }
      }
      BARRIER();
      const int t = wave * 32 + (lane & 31);
      float c0 = 0.f;
#pragma unroll
      for (int w4 = 0; w4 < 4; ++w4) c0 += cx[((lane >> 5) * 4 + w4) * 256 + t];
      c0 = tr32(c0, c0);
      ((u16*)(lds + L_O))[t] = (u16)pkbf(c0, c0);
      BARRIER();
    }
    project_publish(w, lds, rws, WS_OUTP + (32 + wi) * 7168, tg, wave, lane);
    if (wave < 7) {
      const int f = wi * 7 + wave;
#pragma unroll 1
      for (int br = 1; br >= 0; --br) {
        const u32 off = WS_OUTP + (br * 32 + (lane >> 1)) * 7168 + (2 * f + (lane & 1)) * 16;
        v4u gv;
#pragma unroll 1
        for (u32 spin = 0; spin < SPIN_CAP; ++spin) {
          CFENCE();
          gv = ld16<AUX_SC1>(rws, off, 0);
          if (all0(gbad(gv, tg))) break;
          __builtin_amdgcn_s_sleep(1);
        }
        float a = tr32(gdec(gv[0]), gdec(gv[2]));
        a = tr16(a, a);
        a += dpp<XOR8>(a);
        a += xor4(a);
        a += dpp<XOR2>(a);
        const float o = dpp<XOR1>(a);
        __builtin_amdgcn_raw_buffer_store_b32(pkbf(a, o), mkrsrc(out, 3584u), (lane & 31) == 0 ? ((lane >> 5) * 896 + br * 448 + 2 * f) * 2 : OOB, 0, 0);
      }
    }
    return;
  }

  float stat_m = 0.f, stat_l = 0.f;
  if (roleC) {
    const int lane = get_lane();
    const rsrc_t rc = mkrsrc(cbase, 128u * 2048u);
    const u32 vo = wave * 1024 + lane * 16;
    v4u buf[3][8];
    bool ready = false;
    const int g4 = lane >> 4, r16 = lane & 15;
    const int pg = 2 * (g4 & 1) + (g4 >> 1);
    const int ktq = wave & 1, cq = wave >> 1;
    const u32 wbase0 = g4 * 8192 + r16 * 16;
#define IMG_ADDR(row_, half_) ((wbase ^ (((((row_) & 7) << 1) | (((row_) >> 3) & 1)) * 16)) + (row_) * 256 + (half_) * 32768)
#define WRITE_IMG(src_) do { char* img_ = lds + L_BIG; u32 wbase = wbase0; asm volatile("" : "+v"(wbase)); \
      _Pragma("unroll") for (int i_ = 0; i_ < 8; ++i_) \
        *(v4u*)(img_ + IMG_ADDR(4 * i_ + (wave >> 1), wave & 1)) = (src_)[i_]; } while (0)
#define STAGE_Q() do { CFENCE(); v4u t_[4]; u32 wbase = wbase0; asm volatile("" : "+v"(wbase)); \
      _Pragma("unroll") for (int i_ = 0; i_ < 4; ++i_) t_[i_] = ld16<AUX_SC1>(rws, WS_QABS + bC * 32768 + wave * 4096 + lane * 16 + i_ * 1024, 0); \
      _Pragma("unroll") for (int i_ = 0; i_ < 4; ++i_) *(v4u*)(lds + L_XQ + IMG_ADDR(2 * wave + (i_ >> 1), i_ & 1)) = t_[i_]; \
      ready = true; } while (0)
    if (wave == 0) {
      WRITE_IMG(wk);
#pragma unroll
      for (int i = 0; i < 24; ++i) buf[(1 + (i >> 3)) % 3][i & 7] = wk[8 + i];
      {
        const u32 offA = WS_QG + (bA * 4096 + hA * 256) * 8 + lane * 16;
#pragma unroll 1
        for (u32 spin = 0; spin < SPIN_CAP; ++spin) {
          CFENCE();
          const v4u a0 = ld16<AUX_SC1>(rws, offA, 0), a1 = ld16<AUX_SC1>(rws, offA, 1024);
          u32* qa = (u32*)(lds + L_QA);
          qa[lane] = gq(a0); qa[64 + lane] = gq(a1);
          if (all0(gbad(a0, tg) | gbad(a1, tg))) break;
          __builtin_amdgcn_s_sleep(POLL_SLEEP);
        }
        WAIT_LGKM();
        ctl[L_QAF / 4] = 1;
      }
      u32 pv[4];
#pragma unroll
      for (int k = 0; k < 4; ++k) pv[k] = WS_F2 + (k * 64 + lane) * 128;
#pragma unroll 1
      for (u32 spin = 0; spin < SPIN_CAP; ++spin) {
        CFENCE();
        v2u f[4];
#pragma unroll
        for (int k = 0; k < 4; ++k) f[k] = ld8<AUX_SC1>(rws, pv[k], 0);
#pragma unroll
        for (int k = 0; k < 4; ++k) pv[k] = ((f[k][0] ^ tl) | (f[k][1] ^ th)) ? pv[k] : OOB;
        if (all0((pv[0] & pv[1] & pv[2] & pv[3]) ^ OOB)) break;
        __builtin_amdgcn_s_sleep(POLL_SLEEP);
      }
      ctl[L_F2 / 4] = 1;
    } else {
#pragma unroll
      for (int rd = 0; rd < 4; ++rd) {
        if (rd == 3) WRITE_IMG(buf[0]);
#pragma unroll
        for (int i = 0; i < 8; ++i) {
          if (rd == 0 && i == EARLY) GET_T();
          if ((i & 1) == 0) {
            WAIT_VM(PACE_C - 2);
            if (rd >= 2 && !ready && __builtin_amdgcn_readfirstlane(ctl[L_F2 / 4]) != 0) STAGE_Q();
          }
          buf[rd % 3][i] = ld16<AUX_CK>(rc, vo, rd * 65536 + i * 8192);
        }
      }
    }
    if (!ready) {
      lds_wait(ctl + L_F2 / 4, 1u);
      STAGE_Q();
    }
    const u32 fr = ((r16 & 7) << 1) | (r16 >> 3);
    const u32 qaddr = cq * 16384 + r16 * 256 + ((pg ^ fr) * 16);
    const u32 raddr0 = qaddr + ktq * 4096;
    const int trow = 4 * g4 + ((lane >> 2) & 3);
    const u32 ft = ((trow & 7) << 1) | (trow >> 3);
    const u32 taddr0 = wave * 8192 + trow * 256 + ((((lane >> 1) & 1) ^ ft) * 16) + (lane & 1) * 8;
    f32x4 acc[8];
#pragma unroll
    for (int t = 0; t < 8; ++t) acc[t] = f32x4{0.f, 0.f, 0.f, 0.f};
    float m = -INFINITY, l = 0.f;
    v4u qf[8];
#pragma unroll
    for (int rd = 0; rd < 4; ++rd) {
      char* img = lds + L_BIG;
      BARRIER();
      if (rd == 0) {
#pragma unroll
        for (int ks = 0; ks < 8; ++ks) qf[ks] = *(const v4u*)(lds + L_XQ + (qaddr ^ ((ks & 3) * 64)) + (ks >> 2) * 8192);
      }
      f32x4 sa = {0.f, 0.f, 0.f, 0.f}, sb = sa;
      u32 raddr = raddr0, taddr = taddr0; asm volatile("" : "+v"(raddr), "+v"(taddr));
#pragma unroll
      for (int ks = 0; ks < 4; ++ks) {
        const v4u a = *(const v4u*)(img + (raddr ^ (ks * 64))), b = *(const v4u*)(img + (raddr ^ (ks * 64)) + 8192);
        sa = mfma(a, qf[ks], sa);
        sb = mfma(b, qf[ks + 4], sb);
      }
      __builtin_amdgcn_sched_barrier(0);
      v4u af[8];
#pragma unroll
      for (int t = 0; t < 8; ++t) {
        const s16x4 a0 = __builtin_amdgcn_ds_read_tr16_b64_v4i16((LAS s16x4*)(img + (taddr ^ (t * 32))));
        const s16x4 a1 = __builtin_amdgcn_ds_read_tr16_b64_v4i16((LAS s16x4*)(img + (taddr ^ (t * 32)) + 4096));
        const v2u u0 = __builtin_bit_cast(v2u, a0), u1 = __builtin_bit_cast(v2u, a1);
        af[t] = v4u{u0[0], u0[1], u1[0], u1[1]};
      }
      char* X = lds + L_XQ + 4096 + lane * 16;
      *(f32x4*)(X + wave * 8192) = sa + sb;
      BARRIER();
      f32x4 sc[2];
      sc[0] = (*(const f32x4*)(X) + *(const f32x4*)(X + 16384)) + (*(const f32x4*)(X + 32768) + *(const f32x4*)(X + 49152));
      __builtin_amdgcn_sched_barrier(0);
      sc[1] = (*(const f32x4*)(X + 8192) + *(const f32x4*)(X + 24576)) + (*(const f32x4*)(X + 40960) + *(const f32x4*)(X + 57344));
      asm volatile("" : "+v"(sc[0]), "+v"(sc[1]));
      if (rd < 3) WRITE_IMG(buf[(rd + 1) % 3]);
      CFENCE();
      float mu = fmaxf(fmaxf(fmaxf(sc[0][0], sc[0][1]), fmaxf(sc[0][2], sc[0][3])), fmaxf(fmaxf(sc[1][0], sc[1][1]), fmaxf(sc[1][2], sc[1][3])));
      mu = max16(mu); mu = max32(mu);
      const float mn = fmaxf(m, mu);
      const float al = ex2((m - mn) * CEXP);
      m = mn;
      float p[8];
#pragma unroll
      for (int e = 0; e < 8; ++e) p[e] = ex2((sc[e >> 2][e & 3] - mn) * CEXP);
      l = fmaf(l, al, ((p[0] + p[1]) + (p[2] + p[3])) + ((p[4] + p[5]) + (p[6] + p[7])));
      const v4u pf = {pkbf(p[0], p[1]), pkbf(p[2], p[3]), pkbf(p[4], p[5]), pkbf(p[6], p[7])};
#pragma unroll
      for (int t = 0; t < 8; ++t) {
        if (rd > 0) acc[t] *= al;
        acc[t] = mfma(af[t], pf, acc[t]);
      }
#pragma unroll
      for (int t = 0; t < 8; ++t) asm volatile("" : "+v"(acc[t]));
    }
    l = tr16(l, l); l = tr32(l, l);
    const float inv = 1.f / l;
    {
      char* tb = lds + L_BIG + wave * 8192;
#pragma unroll
      for (int tp = 0; tp < 4; ++tp) {
        const f32x4 a = acc[2 * tp] * inv, b = acc[2 * tp + 1] * inv;
        *(v4u*)(tb + r16 * 272 + tp * 64 + g4 * 16) = v4u{pkbf(a[0], a[1]), pkbf(a[2], a[3]), pkbf(b[0], b[1]), pkbf(b[2], b[3])};
      }
      WAIT_LGKM();
      const u32 dst = WS_P3 + ((bC * 32 + sC) * 16 + g4) * 2048 + wave * 256 + r16 * 16;
#pragma unroll
      for (int k = 0; k < 4; ++k) __builtin_amdgcn_raw_buffer_store_b128(*(const v4u*)(tb + (4 * k + g4) * 272 + r16 * 16), rws, dst + k * 8192, 0, AUX_SC1);
    }
    stat_m = gdec(genc(m, tg)); stat_l = l * ex2((m - stat_m) * CEXP);
    if (r < 4) {
      const int pi = xc * 4 + r, hP = pi >> 1, dh = pi & 1;
      WAIT_VM(0);
      BARRIER();
      if (wave == 0) st16(v4u{genc(stat_m, tg), tg.hi, genc(stat_l, tg), tg.hi}, rws, lane < 16 ? WS_ST3 + lane * 1024 + (bC * 32 + sC) * 16 : OOB);
      v4u w[16];
      load_proj<PACE_C, AUX_AO>(w, Awo + (size_t)(pi * 128) * 448, wave, lane);
      to_frags(&w[0]); to_frags(&w[8]);
      const int jq = lane >> 4, bq = (lane >> 3) & 1, pr = lane & 7;
      const bool two = jq < 2;
      const u32 rec = (hP * 2 + bq) * 8 + jq;
      const u32 oo = WS_OA + rec * OA_REC + (dh * 128 + wave * 16 + 2 * pr) * 8, os = WS_OA + rec * OA_REC + 2048;
      const u32 oo2 = two ? oo + 4 * OA_REC : oo, os2 = two ? os + 4 * OA_REC : os;
      v4u a0, a1, s0, s1;
#pragma unroll 1
      for (u32 spin = 0; spin < SPIN_CAP; ++spin) {
        CFENCE();
        a0 = ld16<AUX_SC1>(rws, oo, 0); a1 = ld16<AUX_SC1>(rws, oo2, 0); s0 = ld16<AUX_SC1>(rws, os, 0); s1 = ld16<AUX_SC1>(rws, os2, 0);
        if (all0(gbad(a0, tg) | gbad(a1, tg) | gbad(s0, tg) | gbad(s1, tg))) break;
        __builtin_amdgcn_s_sleep(1);
      }
      const float m0 = gdec(s0[0]), m1 = gdec(s1[0]);
      float M = fmaxf(m0, m1); M = max16(M); M = max32(M);
      const float e0 = ex2((m0 - M) * CEXP), e1 = two ? ex2((m1 - M) * CEXP) : 0.f;
      float L = e0 * gdec(s0[2]) + e1 * gdec(s1[2]);
      float o0 = e0 * gdec(a0[0]) + e1 * gdec(a1[0]), o1 = e0 * gdec(a0[2]) + e1 * gdec(a1[2]);
      L = tr16(L, L); L = tr32(L, L);
      o0 = tr16(o0, o1);
      o0 = tr32(o0, o0) * (1.f / L);
      ((u16*)(lds + L_O))[bq * 128 + wave * 16 + 2 * pr + (jq & 1)] = (u16)pkbf(o0, o0);
      BARRIER();
      project_publish(w, lds, rws, WS_OUTP + pi * 7168, tg, wave, lane);
      return;
    }
  }

  {
    const int lane = get_lane();
    constexpr int N35 = 512 - UNITS_C1 - 5 * UNITS_W0;
    const size_t base = (size_t)(bA * 16 + hA) * 4096 * 256;
    const rsrc_t rk = mkrsrc(Ak + base, 512u * 4096u), rv = mkrsrc(Av + base, 512u * 4096u);
    const int w35 = (wave - 1) * 5 + ja;
    const int first = roleC ? 512 - UNITS_C1 + wave : (wave ? w35 : N35 + ja), stride = roleC ? 8 : (wave ? 35 : 5);
    const int cnt = roleC ? (UNITS_C1 + 7 - wave) / 8 : (wave ? (N35 + 34 - w35) / 35 : UNITS_W0);
#define UOFF(u) ((u32)(u) * 4096u)
    const bool late = roleC || wave == 0;
    const u32 vo = lane * 16;
    v4u kb[3][4], vb[3][4];
#pragma unroll
    for (int s = 0; s < 3; ++s) {
      const u32 so = UOFF(first + stride * s);
#pragma unroll
      for (int i = 0; i < 8; ++i) {
        if (s == 0 && i == EARLY) GET_T();
        if ((i & 1) == 0 && s * 8 + i >= 4) { if (late) WAIT_VM(PACE_A - 2); else WAIT_VM(PACE_A0 - 2); }
        if (i < 4) kb[s][i] = ld16<AUX_NT>(rk, vo + i * 1024, so);
        else vb[s][i - 4] = ld16<AUX_NT>(rv, vo + (i - 4) * 1024, so);
        if (s == 0 && i == 3 && roleC) {
          WAIT_VM(4);
          BARRIER();
          if (wave == 0) {
            st16(v4u{genc(stat_m, tg), tg.hi, genc(stat_l, tg), tg.hi}, rws, lane < 16 ? WS_ST3 + lane * 1024 + (bC * 32 + sC) * 16 : OOB);
          }
        }
      }
    }
    lds_wait(ctl + L_QAF / 4, 1u);
    const v4u q = *(const v4u*)(lds + L_QA + (lane & 31) * 16);
    float m = -INFINITY, l = 0.f, acc[8];
#pragma unroll
    for (int k = 0; k < 8; ++k) acc[k] = 0.f;
#define UNIT(S, REFILL, so) do { \
      const float a_ = tr16(dot8(kb[S][0], q), dot8(kb[S][2], q)), b_ = tr16(dot8(kb[S][1], q), dot8(kb[S][3], q)); \
      if (REFILL) { __builtin_amdgcn_sched_barrier(0); _Pragma("unroll") for (int i = 0; i < 4; ++i) { if ((i & 1) == 0) WAIT_VM(PACE_A - 2); kb[S][i] = ld16<AUX_NT>(rk, vo + i * 1024, so); } } \
      const float t1_ = a_ + dpp<XOR8>(a_), t2_ = b_ + dpp<XOR8>(b_); \
      float y_ = (lane & 8) ? t2_ : t1_; \
      y_ += dpp<XOR7>(y_); y_ += dpp<XOR2>(y_); y_ += dpp<XOR1>(y_); \
      y_ = rbf(y_);                                               \
      float mu_ = fmaxf(y_, dpp<XOR8>(y_)); mu_ = max16(mu_); mu_ = max32(mu_); \
      if (__builtin_amdgcn_readfirstlane(mu_ > m ? 1 : 0)) { \
        const float al_ = ex2((m - mu_) * CEXP); \
        l *= al_; \
        _Pragma("unroll") for (int e = 0; e < 8; ++e) acc[e] *= al_; \
        m = mu_; \
      } \
      const float p_ = ex2((y_ - m) * CEXP); \
      l += p_; \
      const float px_ = dpp<XOR8>(p_); \
      const float pe_ = (lane & 8) ? px_ : p_, po_ = (lane & 8) ? p_ : px_; \
      auto se_ = __builtin_amdgcn_permlane16_swap(__float_as_uint(pe_), __float_as_uint(pe_), false, false); \
      auto so_ = __builtin_amdgcn_permlane16_swap(__float_as_uint(po_), __float_as_uint(po_), false, false); \
      _Pragma("unroll") for (int i = 0; i < 4; ++i) { \
        const float pi_ = __uint_as_float(i == 0 ? se_[0] : i == 1 ? so_[0] : i == 2 ? se_[1] : so_[1]); \
        _Pragma("unroll") for (int e = 0; e < 4; ++e) { \
          acc[2 * e] = fmaf(pi_, blo(vb[S][i][e]), acc[2 * e]); \
          acc[2 * e + 1] = fmaf(pi_, bhi(vb[S][i][e]), acc[2 * e + 1]); \
        } \
        if (REFILL && (i & 1)) { asm volatile("" : "+v"(acc[0]), "+v"(acc[1]), "+v"(acc[2]), "+v"(acc[3]), "+v"(acc[4]), "+v"(acc[5]), "+v"(acc[6]), "+v"(acc[7])); \
          WAIT_VM(PACE_A - 2); vb[S][i - 1] = ld16<AUX_NT>(rv, vo + (i - 1) * 1024, so); vb[S][i] = ld16<AUX_NT>(rv, vo + i * 1024, so); } \
      } \
    } while (0)
    UNIT(0, true, cnt > 3 ? UOFF(first + stride * 3) : OOB);
    UNIT(1, false, 0u);
    UNIT(2, false, 0u);
#pragma unroll 1
    for (int k = 4; k < cnt; ++k) { UNIT(0, true, UOFF(first + stride * k)); }
    if (cnt > 3) UNIT(0, false, 0u);
#undef UNIT
#pragma unroll
    for (int e = 0; e < 4; ++e) acc[e] = tr32(acc[e], acc[e + 4]);
    l = tr32(l, l); l = tr16(l, l); l += dpp<XOR8>(l);
    float* mrg = (float*)(lds + L_BIG);
    *(f32x4*)(mrg + wave * 260 + 4 + (lane & 31) * 8 + (lane >> 5) * 4) = f32x4{acc[0], acc[1], acc[2], acc[3]};
    if (lane == 0) *(f32x2*)(mrg + wave * 260) = f32x2{m, l};
    BARRIER();
    if (wave < 2) {
      const int t = wave * 64 + lane;
      float M = -INFINITY;
#pragma unroll
      for (int w = 0; w < 8; ++w) M = fmaxf(M, mrg[w * 260]);
      float L = 0.f, o0 = 0.f, o1 = 0.f;
#pragma unroll
      for (int w = 0; w < 8; ++w) {
        const f32x2 ml = *(const f32x2*)(mrg + w * 260), ov = *(const f32x2*)(mrg + w * 260 + 4 + 2 * t);
        const float e = ex2((ml[0] - M) * CEXP);
        L = fmaf(e, ml[1], L); o0 = fmaf(e, ov[0], o0); o1 = fmaf(e, ov[1], o1);
      }
      const u32 rec = (hA * 2 + bA) * 8 + ja;
      st16(v4u{genc(o0, tg), tg.hi, genc(o1, tg), tg.hi}, rws, WS_OA + rec * OA_REC + t * 16);
      if (t == 0) st16(v4u{genc(M, tg), tg.hi, genc(L, tg), tg.hi}, rws, WS_OA + rec * OA_REC + 2048);
    }
  }
}

extern "C" void kernel_launch(void* const* d_in, const int* in_sizes, int n_in, void* d_out, int out_size, void* d_ws,
                              size_t ws_size, hipStream_t stream) {
  fused_decode<<<256, 512, 0, stream>>>((const u16*)d_in[0], (const u16*)d_in[1], (const u16*)d_in[2], (const u16*)d_in[3],
                                        (const u16*)d_in[4], (const u16*)d_in[5], (const u16*)d_in[6], (const u16*)d_in[7],
                                        (const u16*)d_in[8], (const u16*)d_in[9], (const u16*)d_in[10], (u16*)d_out, (char*)d_ws);
}
```
